```python
import torch

def _softmax(x, axis=-1):
    return torch.softmax(x, dim=axis)

def _gelu(x):
    return torch.nn.functional.gelu(x, approximate='tanh')

def _silu(x):
    return torch.nn.functional.silu(x)

def _sigmoid(x):
    return torch.sigmoid(x)

def _layernorm(x, axis=-1, eps=1e-05):
    m = x.mean(dim=axis, keepdim=True)
    v = x.var(dim=axis, keepdim=True, unbiased=False)
    return (x - m) / torch.sqrt(v + eps)

def _scatter_add(tgt, idx, src, axis=0):
    return tgt.clone().index_add_(axis, idx, src)

def _segment_sum(src, seg, n):
    out = torch.zeros((n,) + src.shape[1:], dtype=src.dtype)
    return out.index_add_(0, seg, src)

def _pad(x, pw):
    flat = tuple((v for pair in reversed(pw) for v in pair))
    return torch.nn.functional.pad(x, flat)
kernel_name = 'parallel_sum__memory_aug_recurrent_mem_tr__moe_arch_mod__prompt'

def setup_inputs():

    _w = lambda a: (lambda t: t.to(torch.bfloat16) if t.is_floating_point() else t)(torch.as_tensor(a))

    def _A_setup():
        B, S, Dm = (1, 2048, 512)
        H, Dh = (32, 128)
        M, seg = (8, 32)
        seg = min(seg, S)
        S = S // seg * seg
        return {'x': _w((lambda *s: torch.randn(*s, dtype=torch.bfloat16) * 0.05)(B, S, Dm)), 'mem0': _w((lambda *s: torch.randn(*s, dtype=torch.bfloat16) * 1.6)(B, M, Dm)), 'Wq': _w((lambda *s: torch.randn(*s, dtype=torch.bfloat16) * 0.8)(Dm, H, Dh) * Dm ** (-0.5)), 'Wk': _w((lambda *s: torch.randn(*s, dtype=torch.bfloat16) * 0.8)(Dm, H, Dh) * Dm ** (-0.5)), 'Wv': _w((lambda *s: torch.randn(*s, dtype=torch.bfloat16) * 1.6)(Dm, H, Dh) * Dm ** (-0.5)), 'Wo': _w((lambda *s: torch.randn(*s, dtype=torch.bfloat16) * 1.6)(H, Dh, Dm) * (H * Dh) ** (-0.5)), 'seg_len': seg}
    _A_in = _A_setup()

    def _B_setup():
        B, S, D, F, C = (1, 2048, 512, 1792, 2)
        return {'x': _w((lambda *s: torch.randn(*s, dtype=torch.bfloat16) * 0.05)(B, S, D)), 'w_r': _w((lambda *s: torch.randn(*s, dtype=torch.bfloat16) * 0.05)(D)), 'w_gate': _w((lambda *s: torch.randn(*s, dtype=torch.bfloat16) * 0.05)(D, F)), 'w_up': _w((lambda *s: torch.randn(*s, dtype=torch.bfloat16) * 0.05)(D, F)), 'w_down': _w((lambda *s: torch.randn(*s, dtype=torch.bfloat16) * 0.05)(F, D))}
    _B_in = _B_setup()
    return {'A_x': _A_in['x'], 'A_mem0': _A_in['mem0'], 'A_Wq': _A_in['Wq'], 'A_Wk': _A_in['Wk'], 'A_Wv': _A_in['Wv'], 'A_Wo': _A_in['Wo'], 'A_seg_len': _A_in['seg_len'], 'B_x': _B_in['x'], 'B_w_r': _B_in['w_r'], 'B_w_gate': _B_in['w_gate'], 'B_w_up': _B_in['w_up'], 'B_w_down': _B_in['w_down']}

def reference(A_x, A_mem0, A_Wq, A_Wk, A_Wv, A_Wo, A_seg_len, B_x, B_w_r, B_w_gate, B_w_up, B_w_down):

    def _A_ref(x, mem0, Wq, Wk, Wv, Wo, seg_len):
        B, S, Dm = x.shape
        H, Dh = (Wq.shape[1], Wq.shape[2])
        M = mem0.shape[1]
        scale = Dh ** (-0.5)
        n_seg = S // seg_len
        L = M + seg_len
        idx = torch.arange(L)
        mask = (idx[:, None] < M) | (idx[:, None] >= idx[None, :])
        mem = mem0
        outs = []
        for s in range(n_seg):
            xs = x[:, s * seg_len:(s + 1) * seg_len, :].clone()
            h = torch.concatenate([mem, xs], axis=1)
            q = torch.einsum('btd,dhe->bhte', h, Wq)
            k = torch.einsum('btd,dhe->bhte', h, Wk)
            v = torch.einsum('btd,dhe->bhte', h, Wv)
            scores = torch.einsum('bhqe,bhke->bhqk', q, k) * scale
            scores = torch.where(mask[None, None], scores, -1000000000.0)
            p = _softmax(scores, axis=-1)
            o = torch.einsum('bhqk,bhke->bhqe', p, v)
            o = torch.einsum('bhte,hed->btd', o, Wo)
            mem = o[:, :M, :].clone()
            outs.append(o[:, M:, :])
        return torch.concatenate(outs, axis=1)

    def _B_ref(x, w_r, w_gate, w_up, w_down):
        B, S, D = x.shape
        C = 2
        r = x @ w_r
        idx = torch.argsort(-r, axis=-1)[:, :C]
        idx_e = (lambda x, n, axis: torch.repeat_interleave(x, n, dim=axis))(idx.reshape(B, C, 1), D, axis=2)
        x_sel = (lambda x, i, axis: torch.take_along_dim(x, i, dim=axis))(x, idx_e, axis=1)
        g = _silu(x_sel @ w_gate)
        u = x_sel @ w_up
        y = g * u @ w_down
        r_sel = _sigmoid((lambda x, i, axis: torch.take_along_dim(x, i, dim=axis))(r, idx, axis=1))
        y = y * r_sel.reshape(B, C, 1)
        b_off = (torch.arange(B) * S).reshape(B, 1)
        flat_idx = (idx + b_off).reshape(B * C)
        zero = (x * 0.0).reshape(B * S, D)
        upd = _scatter_add(zero, flat_idx, y.reshape(B * C, D), axis=0)
        return x + upd.reshape(B, S, D)
    x = A_x
    xn = _layernorm(x, axis=-1)
    return x + _A_ref(xn, A_mem0, A_Wq, A_Wk, A_Wv, A_Wo, A_seg_len) + _B_ref(xn, B_w_r, B_w_gate, B_w_up, B_w_down)
```

```cpp
#include <hip/hip_runtime.h>
#include <hip/hip_bf16.h>
#include <stdint.h>

typedef unsigned short u16;
__device__ __forceinline__ float bf2f(u16 b) { return __uint_as_float((unsigned)b << 16); }
__device__ __forceinline__ u16 f2bf(float f) { unsigned u = __float_as_uint(f); u += 0x7FFFu + ((u >> 16) & 1u); return (u16)(u >> 16); }
__device__ __forceinline__ float rbf(float f) { return bf2f(f2bf(f)); }

constexpr int S = 2048, D = 512, H = 32, DH = 128, M = 8, SEG = 32, NSEG = 64, L = 40, F = 1792, HD = 4096;

__global__ void k_ln(const u16* x, const u16* wr, u16* xn, float* r) {
    const int row = blockIdx.x, t = threadIdx.x;
    float v[8]; float s = 0.f;
    for (int j = 0; j < 8; ++j) { v[j] = bf2f(x[row * D + t * 8 + j]); s += v[j]; }
    for (int d = 32; d; d >>= 1) s += __shfl_xor(s, d);
    const float mean = s / D;
    float q = 0.f;
    for (int j = 0; j < 8; ++j) { float d = v[j] - mean; q += d * d; }
    for (int d = 32; d; d >>= 1) q += __shfl_xor(q, d);
    const float mb = rbf(mean), vb = rbf(q / D);
    const float den = rbf(sqrtf(rbf(vb + 1e-5f)));
    float rr = 0.f;
    for (int j = 0; j < 8; ++j) { float y = rbf(rbf(v[j] - mb) / den); xn[row * D + t * 8 + j] = f2bf(y); rr += y * bf2f(wr[t * 8 + j]); }
    for (int d = 32; d; d >>= 1) rr += __shfl_xor(rr, d);
    if (t == 0) r[row] = rbf(rr);
}
__global__ void k_top2(const float* r, int* idx) {
    __shared__ float sv[256]; __shared__ int si[256];
    for (int pass = 0; pass < 2; ++pass) {
        float bv = -INFINITY; int bi = 0x7fffffff;
        for (int i = threadIdx.x; i < S; i += 256) { if (pass == 1 && i == idx[0]) continue; float v = r[i]; if (v > bv || (v == bv && i < bi)) { bv = v; bi = i; } }
        sv[threadIdx.x] = bv; si[threadIdx.x] = bi; __syncthreads();
        if (threadIdx.x == 0) { for (int i = 1; i < 256; ++i) if (sv[i] > bv || (sv[i] == bv && si[i] < bi)) { bv = sv[i]; bi = si[i]; } idx[pass] = bi; }
        __threadfence(); __syncthreads();
    }
}
__global__ void k_moe_gu(const u16* xn, const int* idx, const u16* wg, const u16* wu, u16* hh) {
    const int f = blockIdx.x * blockDim.x + threadIdx.x, c = blockIdx.y; if (f >= F) return;
    const u16* xr = xn + (size_t)idx[c] * D; float g = 0, u = 0;
    for (int d = 0; d < D; ++d) { float xv = bf2f(xr[d]); g += xv * bf2f(wg[d * F + f]); u += xv * bf2f(wu[d * F + f]); }
    g = rbf(g); u = rbf(u);
    const float sg = rbf(g / (1.f + expf(-g)));
    hh[c * F + f] = f2bf(sg * u);
}
__global__ void k_moe_dn(const u16* hh, const int* idx, const float* r, const u16* wd, float* y) {
    const int d = blockIdx.x * blockDim.x + threadIdx.x, c = blockIdx.y; float a = 0;
    for (int f = 0; f < F; ++f) a += bf2f(hh[c * F + f]) * bf2f(wd[f * D + d]);
    a = rbf(a);
    const float rs = rbf(1.f / (1.f + expf(-r[idx[c]])));
    y[c * D + d] = rbf(a * rs);
}
__global__ void k_gemm(const u16* A, const u16* B, u16* C, int K, int N) {
    const int n = blockIdx.x * blockDim.x + threadIdx.x, r0 = blockIdx.y * 8;
    float acc[8] = {0};
    for (int k = 0; k < K; ++k) { float b = bf2f(B[(size_t)k * N + n]); for (int j = 0; j < 8; ++j) acc[j] += bf2f(A[(size_t)(r0 + j) * K + k]) * b; }
    for (int j = 0; j < 8; ++j) C[(size_t)(r0 + j) * N + n] = f2bf(acc[j]);
}
__global__ void k_attn(const u16* Q, const u16* K, const u16* V, const u16* qm, const u16* km, const u16* vm, u16* o, int s) {
    const int h = blockIdx.x, i = blockIdx.y, t = threadIdx.x;
    __shared__ float sc[L]; __shared__ float p[L];
    const u16* q = (i < M) ? qm + i * HD + h * DH : Q + (size_t)(s * SEG + i - M) * HD + h * DH;
    if (t < L) {
        const u16* k = (t < M) ? km + t * HD + h * DH : K + (size_t)(s * SEG + t - M) * HD + h * DH;
        float a = 0; for (int e = 0; e < DH; ++e) a += bf2f(q[e]) * bf2f(k[e]);
        a = rbf(rbf(a) * 0.08838834764831845f);
        const bool ok = (i < M) || (i >= t);
        sc[t] = ok ? a : -1e9f;
    }
    __syncthreads();
    float mx = -INFINITY; for (int j = 0; j < L; ++j) mx = fmaxf(mx, sc[j]);
    float sum = 0; for (int j = 0; j < L; ++j) sum += expf(sc[j] - mx);
    if (t < L) p[t] = rbf(expf(sc[t] - mx) / sum);
    __syncthreads();
    float a = 0;
    for (int j = 0; j < L; ++j) { const u16* v = (j < M) ? vm + j * HD + h * DH : V + (size_t)(s * SEG + j - M) * HD + h * DH; a += p[j] * bf2f(v[t]); }
    o[i * HD + h * DH + t] = f2bf(a);
}
__global__ void k_final(const u16* x, const u16* xn, const u16* oo  , const int* idx, const float* y, u16* out, u16* mem, int s) {
    const int i = blockIdx.x, d = threadIdx.x + blockIdx.y * 256;
    if (i < M) { mem[i * D + d] = oo[i * D + d]; return; }
    const int row = s * SEG + i - M;
    float b = bf2f(xn[row * D + d]); float upd = 0.f;
    if (row == idx[0]) upd = rbf(upd + y[d]);
    if (row == idx[1]) upd = rbf(upd + y[D + d]);
    b = rbf(b + upd);
    const float a = rbf(bf2f(x[row * D + d]) + bf2f(oo[i * D + d]));
    out[row * D + d] = f2bf(a + b);
}

extern "C" void kernel_launch(void* const* d_in, const int* in_sizes, int n_in, void* d_out, int out_size, void* d_ws, size_t ws_size, hipStream_t stream) {
    const u16* x = (const u16*)d_in[0]; const u16* mem0 = (const u16*)d_in[1];
    const u16* Wq = (const u16*)d_in[2]; const u16* Wk = (const u16*)d_in[3]; const u16* Wv = (const u16*)d_in[4]; const u16* Wo = (const u16*)d_in[5];
    const u16* wr = (const u16*)d_in[8]; const u16* wg = (const u16*)d_in[9]; const u16* wu = (const u16*)d_in[10]; const u16* wd = (const u16*)d_in[11];
    u16* out = (u16*)d_out;
    char* ws = (char*)d_ws; size_t off = 0;
    auto alloc = [&](size_t b) { char* p = ws + off; off += (b + 255) & ~(size_t)255; return p; };
    u16* xn = (u16*)alloc(S * D * 2); float* r = (float*)alloc(S * 4); int* idx = (int*)alloc(64); u16* hh = (u16*)alloc(2 * F * 2); float* y = (float*)alloc(2 * D * 4);
    u16* Q = (u16*)alloc((size_t)S * HD * 2); u16* K = (u16*)alloc((size_t)S * HD * 2); u16* V = (u16*)alloc((size_t)S * HD * 2);
    u16* qm = (u16*)alloc(M * HD * 2); u16* km = (u16*)alloc(M * HD * 2); u16* vm = (u16*)alloc(M * HD * 2);
    u16* o = (u16*)alloc(L * HD * 2); u16* oo = (u16*)alloc(L * D * 2); u16* mem = (u16*)alloc(M * D * 2);
    k_ln<<<S, 64, 0, stream>>>(x, wr, xn, r);
    k_top2<<<1, 256, 0, stream>>>(r, idx);
    k_moe_gu<<<dim3(7, 2), 256, 0, stream>>>(xn, idx, wg, wu, hh);
    k_moe_dn<<<dim3(2, 2), 256, 0, stream>>>(hh, idx, r, wd, y);
    k_gemm<<<dim3(HD / 256, S / 8), 256, 0, stream>>>(xn, Wq, Q, D, HD);
    k_gemm<<<dim3(HD / 256, S / 8), 256, 0, stream>>>(xn, Wk, K, D, HD);
    k_gemm<<<dim3(HD / 256, S / 8), 256, 0, stream>>>(xn, Wv, V, D, HD);
    hipMemcpyAsync(mem, mem0, M * D * 2, hipMemcpyDeviceToDevice, stream);
    for (int s = 0; s < NSEG; ++s) {
        k_gemm<<<dim3(HD / 256, 1), 256, 0, stream>>>(mem, Wq, qm, D, HD);
        k_gemm<<<dim3(HD / 256, 1), 256, 0, stream>>>(mem, Wk, km, D, HD);
        k_gemm<<<dim3(HD / 256, 1), 256, 0, stream>>>(mem, Wv, vm, D, HD);
        k_attn<<<dim3(H, L), 128, 0, stream>>>(Q, K, V, qm, km, vm, o, s);
        k_gemm<<<dim3(D / 256, L / 8), 256, 0, stream>>>(o, Wo, oo, HD, D);
        k_final<<<dim3(L, 2), 256, 0, stream>>>(x, xn, oo, idx, y, out, mem, s);
    }
}
```

```cpp
#include <hip/hip_runtime.h>
#include <stdint.h>

typedef unsigned short u16;
typedef unsigned int u32;
typedef u32 u32x2 __attribute__((ext_vector_type(2)));
typedef u32 u32x4 __attribute__((ext_vector_type(4)));
typedef u32 u32x8 __attribute__((ext_vector_type(8)));
typedef float f32x2 __attribute__((ext_vector_type(2)));
typedef float f32x4 __attribute__((ext_vector_type(4)));
typedef __bf16 bf16x2 __attribute__((ext_vector_type(2)));
typedef __bf16 bf16x8 __attribute__((ext_vector_type(8)));
typedef __bf16 bf16x16 __attribute__((ext_vector_type(16)));
typedef short s16x4 __attribute__((ext_vector_type(4)));
#define GAS __attribute__((address_space(1)))
#define LAS __attribute__((address_space(3)))
typedef GAS u32 gu32;
#define RLX_AGENT __ATOMIC_RELAXED, __HIP_MEMORY_SCOPE_AGENT

constexpr int S_TOK = 2048, DM = 512, NH = 32, DH = 128, MM = 8, SEG = 32, NSEG = 64, FF = 1792, HD = 4096;

constexpr u32 MB = 1u << 20;
constexpr u32 W_MEM  = 0;
constexpr u32 W_RING0 = 1 * MB;
constexpr u32 W_RING1 = 2 * MB;
constexpr u32 W_WIT = 3 * MB;
constexpr u32 W_VMF = 3 * MB + 65536;
constexpr u32 W_NEAR_END = 3 * MB + 65536 + 524288;
constexpr u32 W_DUMMY = 4 * MB + 160 * 1024;
constexpr u32 W_FLAG = 4 * MB;
constexpr u32 FLAG_BYTES = 256 * 1024;
constexpr u32 W_XN = 5 * MB;
constexpr u32 W_XF = 7 * MB;
constexpr u32 W_R  = 9 * MB;
constexpr u32 W_MOE = 9 * MB + 65536;
constexpr u32 W_KM = 10 * MB;
constexpr u32 W_VM = 18 * MB;
constexpr u32 W_QX = 32 * MB, W_KX = 48 * MB, W_VX = 64 * MB, W_OX = 80 * MB;
constexpr u32 W_END = 112 * MB;

constexpr u32 F_CTRL = 0;
constexpr u32 F_CLAIM = 64;
constexpr u32 F_SH = 1024;
constexpr u32 F_TT = F_SH + (NSEG + 3) * NH * 16;
constexpr u32 F_MOE = F_TT + 128 * 16;
constexpr u32 W_SEL = 9 * MB + 32768;

__device__ __forceinline__ float bf2f(u16 b) { return __uint_as_float((u32)b << 16); }
__device__ __forceinline__ u32 cvtpk(float lo, float hi) { f32x2 x = {lo, hi}; return __builtin_bit_cast(u32, __builtin_convertvector(x, bf16x2)); }
__device__ __forceinline__ float rbf(float f) { return __uint_as_float(cvtpk(f, f) << 16); }
__device__ __forceinline__ u32 xcc_id() { return (u32)__builtin_amdgcn_s_getreg(20 | (3 << 11)) & 0xfu; }
#define AUX_SC1 16
#define RSRC_W3 0x00020000
constexpr u32 L_KX = 0, L_VX = 16384, L_QK = 32768, L_O = 36864, L_DUMP = 38912, L_SEL = 39936, L_STG = 40960, L_TAB = 49152, L_MISC = 65536;
constexpr u32 LDS_BYTES = 65536 + 256;
struct Params {
    const u16* x; const u16* mem0; const u16* Wq; const u16* Wk; const u16* Wv; const u16* Wo;
    const u16* wr; const u16* wg; const u16* wu; const u16* wd;
    u16* out; unsigned char* ws;
};
#define HEAD_ASM \
  "s_mov_b32 s40, %[wslo]\n" \
  "s_mov_b32 s41, %[wshi]\n" \
  "s_mov_b32 s42, %[wssz]\n" \
  "s_mov_b32 s43, 0x00020000\n" \
  "s_mov_b32 s49, %[mem0]\n" \
  "s_mov_b32 s50, %[mem0]\n" \
  "s_mov_b32 s64, %[h]\n" \
  "s_mov_b32 s66, %[w]\n" \
  "s_mov_b32 s78, %[wit]\n" \
  "s_mov_b32 s79, %[vf0]\n" \
  "s_mov_b32 s52, %[ring0]\n" \
  "s_mov_b32 s53, %[ring1]\n" \
  "v_mbcnt_lo_u32_b32 v16, -1, 0\n" \
  "v_mbcnt_hi_u32_b32 v16, -1, v16\n" \
  "v_lshlrev_b32 v16, 2, v16\n" \
  "s_mul_i32 s59, s66, 4096\n" \
  "s_add_u32 s59, s59, 49152\n" \
  "v_add_u32 v16, s59, v16\n" \
  "ds_read_b32 v0, v16 offset:0\n" \
  "ds_read_b32 v1, v16 offset:256\n" \
  "ds_read_b32 v2, v16 offset:512\n" \
  "ds_read_b32 v3, v16 offset:768\n" \
  "ds_read_b32 v4, v16 offset:1024\n" \
  "ds_read_b32 v5, v16 offset:1280\n" \
  "ds_read_b32 v6, v16 offset:1536\n" \
  "ds_read_b32 v7, v16 offset:1792\n" \
  "ds_read_b32 v8, v16 offset:2048\n" \
  "ds_read_b32 v9, v16 offset:2304\n" \
  "ds_read_b32 v17, v16 offset:2560\n" \
  "ds_read_b32 v18, v16 offset:2816\n" \
  "ds_read_b32 v19, v16 offset:3072\n" \
  "s_waitcnt lgkmcnt(0)\n" \
  "s_lshl_b32 s54, s64, 14\n" \
  "s_lshl_b32 s59, s66, 11\n" \
  "s_add_u32 s54, s54, s59\n" \
  "s_lshl_b32 s55, s64, 12\n" \
  "s_lshl_b32 s59, s66, 10\n" \
  "s_add_u32 s55, s55, s59\n" \
  "s_add_u32 s55, s55, 0xa00000\n" \
  "s_add_u32 s79, s79, s59\n" \
  "s_add_u32 s73, s79, 262144\n" \
  "s_lshl_b32 s56, s64, 11\n" \
  "s_lshl_b32 s59, s66, 9\n" \
  "s_add_u32 s56, s56, s59\n" \
  "s_add_u32 s56, s56, 0x1200000\n" \
  "s_lshl_b32 s57, s64, 13\n" \
  "s_lshl_b32 s59, s66, 11\n" \
  "s_add_u32 s57, s57, s59\n" \
  "s_add_u32 s62, s59, 0\n" \
  "s_add_u32 s63, s59, 16384\n" \
  "s_add_u32 s49, s49, s59\n" \
  "s_add_u32 s84, s59, 40960\n" \
  "s_lshl_b32 s58, s64, 6\n" \
  "s_add_u32 s58, s58, 0x401000\n" \
  "s_lshl_b32 s67, s66, 2\n" \
  "s_mov_b32 s48, 0\n" \
  "s_mov_b32 s65, 0\n" \
  "s_lshr_b32 s85, s66, 1\n" \
  "s_lshl_b32 s85, s85, 7\n" \
  "s_and_b32 s83, s66, 1\n" \
  "s_lshl_b32 s83, s83, 11\n" \
  "v_mov_b32 v11, -1\n" \
  "s_add_u32 s59, s66, 0\n" \
  "s_and_b32 s59, s59, 3\n" \
  "s_lshl_b32 s74, s59, 20\n" \
  "s_add_u32 s59, s66, 1\n" \
  "s_and_b32 s59, s59, 3\n" \
  "s_lshl_b32 s75, s59, 20\n" \
  "s_lshl_b32 s59, s59, 11\n" \
  "s_add_u32 s59, s59, 40960\n" \
  "v_add_u32 v13, s59, v0\n" \
  "s_add_u32 s59, s66, 2\n" \
  "s_and_b32 s59, s59, 3\n" \
  "s_lshl_b32 s76, s59, 20\n" \
  "s_lshl_b32 s59, s59, 11\n" \
  "s_add_u32 s59, s59, 40960\n" \
  "v_add_u32 v14, s59, v0\n" \
  "s_add_u32 s59, s66, 3\n" \
  "s_and_b32 s59, s59, 3\n" \
  "s_lshl_b32 s77, s59, 20\n" \
  "s_lshl_b32 s59, s59, 11\n" \
  "s_add_u32 s59, s59, 40960\n" \
  "v_add_u32 v15, s59, v0\n" \
  "s_mov_b32 s80, 0x3e0293ee\n" \
  "s_mov_b32 s81, 0x3e0293ee\n" \
  "s_mov_b32 s68, 0x05040100\n" \
  "s_mov_b32 s69, 0x07060302\n" \
  "s_mov_b32 s44, %[wqklo]\n" \
  "s_mov_b32 s45, %[wqkhi]\n" \
  "s_mov_b32 s46, 0x400000\n" \
  "s_mov_b32 s47, 0x00020000\n" \
  "s_add_u32 s59, s74, 0x0\n" \
  "buffer_load_dwordx2 v[128:129], v18, s[44:47], s59 offen\n" \
  "s_add_u32 s59, s74, 0x2000\n" \
  "buffer_load_dwordx2 v[130:131], v18, s[44:47], s59 offen\n" \
  "s_add_u32 s59, s74, 0x4000\n" \
  "buffer_load_dwordx2 v[132:133], v18, s[44:47], s59 offen\n" \
  "s_add_u32 s59, s74, 0x6000\n" \
  "buffer_load_dwordx2 v[134:135], v18, s[44:47], s59 offen\n" \
  "s_add_u32 s59, s74, 0x10000\n" \
  "buffer_load_dwordx2 v[136:137], v18, s[44:47], s59 offen\n" \
  "s_add_u32 s59, s74, 0x12000\n" \
  "buffer_load_dwordx2 v[138:139], v18, s[44:47], s59 offen\n" \
  "s_add_u32 s59, s74, 0x14000\n" \
  "buffer_load_dwordx2 v[140:141], v18, s[44:47], s59 offen\n" \
  "s_add_u32 s59, s74, 0x16000\n" \
  "buffer_load_dwordx2 v[142:143], v18, s[44:47], s59 offen\n" \
  "s_add_u32 s59, s74, 0x80000\n" \
  "buffer_load_dwordx2 v[144:145], v18, s[44:47], s59 offen\n" \
  "s_add_u32 s59, s74, 0x82000\n" \
  "buffer_load_dwordx2 v[146:147], v18, s[44:47], s59 offen\n" \
  "s_add_u32 s59, s74, 0x84000\n" \
  "buffer_load_dwordx2 v[148:149], v18, s[44:47], s59 offen\n" \
  "s_add_u32 s59, s74, 0x86000\n" \
  "buffer_load_dwordx2 v[150:151], v18, s[44:47], s59 offen\n" \
  "s_add_u32 s59, s74, 0x90000\n" \
  "buffer_load_dwordx2 v[152:153], v18, s[44:47], s59 offen\n" \
  "s_add_u32 s59, s74, 0x92000\n" \
  "buffer_load_dwordx2 v[154:155], v18, s[44:47], s59 offen\n" \
  "s_add_u32 s59, s74, 0x94000\n" \
  "buffer_load_dwordx2 v[156:157], v18, s[44:47], s59 offen\n" \
  "s_add_u32 s59, s74, 0x96000\n" \
  "buffer_load_dwordx2 v[158:159], v18, s[44:47], s59 offen\n" \
  "s_add_u32 s59, s74, 0x8000\n" \
  "buffer_load_dwordx2 v[160:161], v18, s[44:47], s59 offen\n" \
  "s_add_u32 s59, s74, 0xa000\n" \
  "buffer_load_dwordx2 v[162:163], v18, s[44:47], s59 offen\n" \
  "s_add_u32 s59, s74, 0xc000\n" \
  "buffer_load_dwordx2 v[164:165], v18, s[44:47], s59 offen\n" \
  "s_add_u32 s59, s74, 0xe000\n" \
  "buffer_load_dwordx2 v[166:167], v18, s[44:47], s59 offen\n" \
  "s_add_u32 s59, s74, 0x18000\n" \
  "buffer_load_dwordx2 v[168:169], v18, s[44:47], s59 offen\n" \
  "s_add_u32 s59, s74, 0x1a000\n" \
  "buffer_load_dwordx2 v[170:171], v18, s[44:47], s59 offen\n" \
  "s_add_u32 s59, s74, 0x1c000\n" \
  "buffer_load_dwordx2 v[172:173], v18, s[44:47], s59 offen\n" \
  "s_add_u32 s59, s74, 0x1e000\n" \
  "buffer_load_dwordx2 v[174:175], v18, s[44:47], s59 offen\n" \
  "s_add_u32 s59, s74, 0x88000\n" \
  "buffer_load_dwordx2 v[176:177], v18, s[44:47], s59 offen\n" \
  "s_add_u32 s59, s74, 0x8a000\n" \
  "buffer_load_dwordx2 v[178:179], v18, s[44:47], s59 offen\n" \
  "s_add_u32 s59, s74, 0x8c000\n" \
  "buffer_load_dwordx2 v[180:181], v18, s[44:47], s59 offen\n" \
  "s_add_u32 s59, s74, 0x8e000\n" \
  "buffer_load_dwordx2 v[182:183], v18, s[44:47], s59 offen\n" \
  "s_add_u32 s59, s74, 0x98000\n" \
  "buffer_load_dwordx2 v[184:185], v18, s[44:47], s59 offen\n" \
  "s_add_u32 s59, s74, 0x9a000\n" \
  "buffer_load_dwordx2 v[186:187], v18, s[44:47], s59 offen\n" \
  "s_add_u32 s59, s74, 0x9c000\n" \
  "buffer_load_dwordx2 v[188:189], v18, s[44:47], s59 offen\n" \
  "s_add_u32 s59, s74, 0x9e000\n" \
  "buffer_load_dwordx2 v[190:191], v18, s[44:47], s59 offen\n" \
  "s_add_u32 s59, s75, 0x0\n" \
  "buffer_load_dwordx2 v[192:193], v18, s[44:47], s59 offen\n" \
  "s_add_u32 s59, s75, 0x2000\n" \
  "buffer_load_dwordx2 v[194:195], v18, s[44:47], s59 offen\n" \
  "s_add_u32 s59, s75, 0x4000\n" \
  "buffer_load_dwordx2 v[196:197], v18, s[44:47], s59 offen\n" \
  "s_add_u32 s59, s75, 0x6000\n" \
  "buffer_load_dwordx2 v[198:199], v18, s[44:47], s59 offen\n" \
  "s_add_u32 s59, s75, 0x10000\n" \
  "buffer_load_dwordx2 v[200:201], v18, s[44:47], s59 offen\n" \
  "s_add_u32 s59, s75, 0x12000\n" \
  "buffer_load_dwordx2 v[202:203], v18, s[44:47], s59 offen\n" \
  "s_add_u32 s59, s75, 0x14000\n" \
  "buffer_load_dwordx2 v[204:205], v18, s[44:47], s59 offen\n" \
  "s_add_u32 s59, s75, 0x16000\n" \
  "buffer_load_dwordx2 v[206:207], v18, s[44:47], s59 offen\n" \
  "s_add_u32 s59, s75, 0x80000\n" \
  "buffer_load_dwordx2 v[208:209], v18, s[44:47], s59 offen\n" \
  "s_add_u32 s59, s75, 0x82000\n" \
  "buffer_load_dwordx2 v[210:211], v18, s[44:47], s59 offen\n" \
  "s_add_u32 s59, s75, 0x84000\n" \
  "buffer_load_dwordx2 v[212:213], v18, s[44:47], s59 offen\n" \
  "s_add_u32 s59, s75, 0x86000\n" \
  "buffer_load_dwordx2 v[214:215], v18, s[44:47], s59 offen\n" \
  "s_add_u32 s59, s75, 0x90000\n" \
  "buffer_load_dwordx2 v[216:217], v18, s[44:47], s59 offen\n" \
  "s_add_u32 s59, s75, 0x92000\n" \
  "buffer_load_dwordx2 v[218:219], v18, s[44:47], s59 offen\n" \
  "s_add_u32 s59, s75, 0x94000\n" \
  "buffer_load_dwordx2 v[220:221], v18, s[44:47], s59 offen\n" \
  "s_add_u32 s59, s75, 0x96000\n" \
  "buffer_load_dwordx2 v[222:223], v18, s[44:47], s59 offen\n" \
  "s_add_u32 s59, s75, 0x8000\n" \
  "buffer_load_dwordx2 v[224:225], v18, s[44:47], s59 offen\n" \
  "s_add_u32 s59, s75, 0xa000\n" \
  "buffer_load_dwordx2 v[226:227], v18, s[44:47], s59 offen\n" \
  "s_add_u32 s59, s75, 0xc000\n" \
  "buffer_load_dwordx2 v[228:229], v18, s[44:47], s59 offen\n" \
  "s_add_u32 s59, s75, 0xe000\n" \
  "buffer_load_dwordx2 v[230:231], v18, s[44:47], s59 offen\n" \
  "s_add_u32 s59, s75, 0x18000\n" \
  "buffer_load_dwordx2 v[232:233], v18, s[44:47], s59 offen\n" \
  "s_add_u32 s59, s75, 0x1a000\n" \
  "buffer_load_dwordx2 v[234:235], v18, s[44:47], s59 offen\n" \
  "s_add_u32 s59, s75, 0x1c000\n" \
  "buffer_load_dwordx2 v[236:237], v18, s[44:47], s59 offen\n" \
  "s_add_u32 s59, s75, 0x1e000\n" \
  "buffer_load_dwordx2 v[238:239], v18, s[44:47], s59 offen\n" \
  "s_add_u32 s59, s75, 0x88000\n" \
  "buffer_load_dwordx2 v[240:241], v18, s[44:47], s59 offen\n" \
  "s_add_u32 s59, s75, 0x8a000\n" \
  "buffer_load_dwordx2 v[242:243], v18, s[44:47], s59 offen\n" \
  "s_add_u32 s59, s75, 0x8c000\n" \
  "buffer_load_dwordx2 v[244:245], v18, s[44:47], s59 offen\n" \
  "s_add_u32 s59, s75, 0x8e000\n" \
  "buffer_load_dwordx2 v[246:247], v18, s[44:47], s59 offen\n" \
  "s_add_u32 s59, s75, 0x98000\n" \
  "buffer_load_dwordx2 v[248:249], v18, s[44:47], s59 offen\n" \
  "s_add_u32 s59, s75, 0x9a000\n" \
  "buffer_load_dwordx2 v[250:251], v18, s[44:47], s59 offen\n" \
  "s_add_u32 s59, s75, 0x9c000\n" \
  "buffer_load_dwordx2 v[252:253], v18, s[44:47], s59 offen\n" \
  "s_add_u32 s59, s75, 0x9e000\n" \
  "buffer_load_dwordx2 v[254:255], v18, s[44:47], s59 offen\n" \
  "s_waitcnt vmcnt(62)\n" \
  "v_perm_b32 v20, v130, v128, s68\n" \
  "v_perm_b32 v21, v130, v128, s69\n" \
  "v_perm_b32 v22, v131, v129, s68\n" \
  "v_perm_b32 v23, v131, v129, s69\n" \
  "v_accvgpr_write_b32 a0, v20\n" \
  "v_accvgpr_write_b32 a64, v21\n" \
  "v_accvgpr_write_b32 a128, v22\n" \
  "v_accvgpr_write_b32 a192, v23\n" \
  "s_waitcnt vmcnt(60)\n" \
  "v_perm_b32 v20, v134, v132, s68\n" \
  "v_perm_b32 v21, v134, v132, s69\n" \
  "v_perm_b32 v22, v135, v133, s68\n" \
  "v_perm_b32 v23, v135, v133, s69\n" \
  "v_accvgpr_write_b32 a1, v20\n" \
  "v_accvgpr_write_b32 a65, v21\n" \
  "v_accvgpr_write_b32 a129, v22\n" \
  "v_accvgpr_write_b32 a193, v23\n" \
  "s_waitcnt vmcnt(58)\n" \
  "v_perm_b32 v20, v138, v136, s68\n" \
  "v_perm_b32 v21, v138, v136, s69\n" \
  "v_perm_b32 v22, v139, v137, s68\n" \
  "v_perm_b32 v23, v139, v137, s69\n" \
  "v_accvgpr_write_b32 a2, v20\n" \
  "v_accvgpr_write_b32 a66, v21\n" \
  "v_accvgpr_write_b32 a130, v22\n" \
  "v_accvgpr_write_b32 a194, v23\n" \
  "s_waitcnt vmcnt(56)\n" \
  "v_perm_b32 v20, v142, v140, s68\n" \
  "v_perm_b32 v21, v142, v140, s69\n" \
  "v_perm_b32 v22, v143, v141, s68\n" \
  "v_perm_b32 v23, v143, v141, s69\n" \
  "v_accvgpr_write_b32 a3, v20\n" \
  "v_accvgpr_write_b32 a67, v21\n" \
  "v_accvgpr_write_b32 a131, v22\n" \
  "v_accvgpr_write_b32 a195, v23\n" \
  "s_waitcnt vmcnt(54)\n" \
  "v_perm_b32 v20, v146, v144, s68\n" \
  "v_perm_b32 v21, v146, v144, s69\n" \
  "v_perm_b32 v22, v147, v145, s68\n" \
  "v_perm_b32 v23, v147, v145, s69\n" \
  "v_accvgpr_write_b32 a4, v20\n" \
  "v_accvgpr_write_b32 a68, v21\n" \
  "v_accvgpr_write_b32 a132, v22\n" \
  "v_accvgpr_write_b32 a196, v23\n" \
  "s_waitcnt vmcnt(52)\n" \
  "v_perm_b32 v20, v150, v148, s68\n" \
  "v_perm_b32 v21, v150, v148, s69\n" \
  "v_perm_b32 v22, v151, v149, s68\n" \
  "v_perm_b32 v23, v151, v149, s69\n" \
  "v_accvgpr_write_b32 a5, v20\n" \
  "v_accvgpr_write_b32 a69, v21\n" \
  "v_accvgpr_write_b32 a133, v22\n" \
  "v_accvgpr_write_b32 a197, v23\n" \
  "s_waitcnt vmcnt(50)\n" \
  "v_perm_b32 v20, v154, v152, s68\n" \
  "v_perm_b32 v21, v154, v152, s69\n" \
  "v_perm_b32 v22, v155, v153, s68\n" \
  "v_perm_b32 v23, v155, v153, s69\n" \
  "v_accvgpr_write_b32 a6, v20\n" \
  "v_accvgpr_write_b32 a70, v21\n" \
  "v_accvgpr_write_b32 a134, v22\n" \
  "v_accvgpr_write_b32 a198, v23\n" \
  "s_waitcnt vmcnt(48)\n" \
  "v_perm_b32 v20, v158, v156, s68\n" \
  "v_perm_b32 v21, v158, v156, s69\n" \
  "v_perm_b32 v22, v159, v157, s68\n" \
  "v_perm_b32 v23, v159, v157, s69\n" \
  "v_accvgpr_write_b32 a7, v20\n" \
  "v_accvgpr_write_b32 a71, v21\n" \
  "v_accvgpr_write_b32 a135, v22\n" \
  "v_accvgpr_write_b32 a199, v23\n" \
  "s_add_u32 s59, s76, 0x0\n" \
  "buffer_load_dwordx2 v[128:129], v18, s[44:47], s59 offen\n" \
  "s_add_u32 s59, s76, 0x2000\n" \
  "buffer_load_dwordx2 v[130:131], v18, s[44:47], s59 offen\n" \
  "s_add_u32 s59, s76, 0x4000\n" \
  "buffer_load_dwordx2 v[132:133], v18, s[44:47], s59 offen\n" \
  "s_add_u32 s59, s76, 0x6000\n" \
  "buffer_load_dwordx2 v[134:135], v18, s[44:47], s59 offen\n" \
  "s_add_u32 s59, s76, 0x10000\n" \
  "buffer_load_dwordx2 v[136:137], v18, s[44:47], s59 offen\n" \
  "s_add_u32 s59, s76, 0x12000\n" \
  "buffer_load_dwordx2 v[138:139], v18, s[44:47], s59 offen\n" \
  "s_add_u32 s59, s76, 0x14000\n" \
  "buffer_load_dwordx2 v[140:141], v18, s[44:47], s59 offen\n" \
  "s_add_u32 s59, s76, 0x16000\n" \
  "buffer_load_dwordx2 v[142:143], v18, s[44:47], s59 offen\n" \
  "s_add_u32 s59, s76, 0x80000\n" \
  "buffer_load_dwordx2 v[144:145], v18, s[44:47], s59 offen\n" \
  "s_add_u32 s59, s76, 0x82000\n" \
  "buffer_load_dwordx2 v[146:147], v18, s[44:47], s59 offen\n" \
  "s_add_u32 s59, s76, 0x84000\n" \
  "buffer_load_dwordx2 v[148:149], v18, s[44:47], s59 offen\n" \
  "s_add_u32 s59, s76, 0x86000\n" \
  "buffer_load_dwordx2 v[150:151], v18, s[44:47], s59 offen\n" \
  "s_add_u32 s59, s76, 0x90000\n" \
  "buffer_load_dwordx2 v[152:153], v18, s[44:47], s59 offen\n" \
  "s_add_u32 s59, s76, 0x92000\n" \
  "buffer_load_dwordx2 v[154:155], v18, s[44:47], s59 offen\n" \
  "s_add_u32 s59, s76, 0x94000\n" \
  "buffer_load_dwordx2 v[156:157], v18, s[44:47], s59 offen\n" \
  "s_add_u32 s59, s76, 0x96000\n" \
  "buffer_load_dwordx2 v[158:159], v18, s[44:47], s59 offen\n" \
  "s_waitcnt vmcnt(62)\n" \
  "v_perm_b32 v20, v162, v160, s68\n" \
  "v_perm_b32 v21, v162, v160, s69\n" \
  "v_perm_b32 v22, v163, v161, s68\n" \
  "v_perm_b32 v23, v163, v161, s69\n" \
  "v_accvgpr_write_b32 a8, v20\n" \
  "v_accvgpr_write_b32 a72, v21\n" \
  "v_accvgpr_write_b32 a136, v22\n" \
  "v_accvgpr_write_b32 a200, v23\n" \
  "s_waitcnt vmcnt(60)\n" \
  "v_perm_b32 v20, v166, v164, s68\n" \
  "v_perm_b32 v21, v166, v164, s69\n" \
  "v_perm_b32 v22, v167, v165, s68\n" \
  "v_perm_b32 v23, v167, v165, s69\n" \
  "v_accvgpr_write_b32 a9, v20\n" \
  "v_accvgpr_write_b32 a73, v21\n" \
  "v_accvgpr_write_b32 a137, v22\n" \
  "v_accvgpr_write_b32 a201, v23\n" \
  "s_waitcnt vmcnt(58)\n" \
  "v_perm_b32 v20, v170, v168, s68\n" \
  "v_perm_b32 v21, v170, v168, s69\n" \
  "v_perm_b32 v22, v171, v169, s68\n" \
  "v_perm_b32 v23, v171, v169, s69\n" \
  "v_accvgpr_write_b32 a10, v20\n" \
  "v_accvgpr_write_b32 a74, v21\n" \
  "v_accvgpr_write_b32 a138, v22\n" \
  "v_accvgpr_write_b32 a202, v23\n" \
  "s_waitcnt vmcnt(56)\n" \
  "v_perm_b32 v20, v174, v172, s68\n" \
  "v_perm_b32 v21, v174, v172, s69\n" \
  "v_perm_b32 v22, v175, v173, s68\n" \
  "v_perm_b32 v23, v175, v173, s69\n" \
  "v_accvgpr_write_b32 a11, v20\n" \
  "v_accvgpr_write_b32 a75, v21\n" \
  "v_accvgpr_write_b32 a139, v22\n" \
  "v_accvgpr_write_b32 a203, v23\n" \
  "s_waitcnt vmcnt(54)\n" \
  "v_perm_b32 v20, v178, v176, s68\n" \
  "v_perm_b32 v21, v178, v176, s69\n" \
  "v_perm_b32 v22, v179, v177, s68\n" \
  "v_perm_b32 v23, v179, v177, s69\n" \
  "v_accvgpr_write_b32 a12, v20\n" \
  "v_accvgpr_write_b32 a76, v21\n" \
  "v_accvgpr_write_b32 a140, v22\n" \
  "v_accvgpr_write_b32 a204, v23\n" \
  "s_waitcnt vmcnt(52)\n" \
  "v_perm_b32 v20, v182, v180, s68\n" \
  "v_perm_b32 v21, v182, v180, s69\n" \
  "v_perm_b32 v22, v183, v181, s68\n" \
  "v_perm_b32 v23, v183, v181, s69\n" \
  "v_accvgpr_write_b32 a13, v20\n" \
  "v_accvgpr_write_b32 a77, v21\n" \
  "v_accvgpr_write_b32 a141, v22\n" \
  "v_accvgpr_write_b32 a205, v23\n" \
  "s_waitcnt vmcnt(50)\n" \
  "v_perm_b32 v20, v186, v184, s68\n" \
  "v_perm_b32 v21, v186, v184, s69\n" \
  "v_perm_b32 v22, v187, v185, s68\n" \
  "v_perm_b32 v23, v187, v185, s69\n" \
  "v_accvgpr_write_b32 a14, v20\n" \
  "v_accvgpr_write_b32 a78, v21\n" \
  "v_accvgpr_write_b32 a142, v22\n" \
  "v_accvgpr_write_b32 a206, v23\n" \
  "s_waitcnt vmcnt(48)\n" \
  "v_perm_b32 v20, v190, v188, s68\n" \
  "v_perm_b32 v21, v190, v188, s69\n" \
  "v_perm_b32 v22, v191, v189, s68\n" \
  "v_perm_b32 v23, v191, v189, s69\n" \
  "v_accvgpr_write_b32 a15, v20\n" \
  "v_accvgpr_write_b32 a79, v21\n" \
  "v_accvgpr_write_b32 a143, v22\n" \
  "v_accvgpr_write_b32 a207, v23\n" \
  "s_add_u32 s59, s76, 0x8000\n" \
  "buffer_load_dwordx2 v[160:161], v18, s[44:47], s59 offen\n" \
  "s_add_u32 s59, s76, 0xa000\n" \
  "buffer_load_dwordx2 v[162:163], v18, s[44:47], s59 offen\n" \
  "s_add_u32 s59, s76, 0xc000\n" \
  "buffer_load_dwordx2 v[164:165], v18, s[44:47], s59 offen\n" \
  "s_add_u32 s59, s76, 0xe000\n" \
  "buffer_load_dwordx2 v[166:167], v18, s[44:47], s59 offen\n" \
  "s_add_u32 s59, s76, 0x18000\n" \
  "buffer_load_dwordx2 v[168:169], v18, s[44:47], s59 offen\n" \
  "s_add_u32 s59, s76, 0x1a000\n" \
  "buffer_load_dwordx2 v[170:171], v18, s[44:47], s59 offen\n" \
  "s_add_u32 s59, s76, 0x1c000\n" \
  "buffer_load_dwordx2 v[172:173], v18, s[44:47], s59 offen\n" \
  "s_add_u32 s59, s76, 0x1e000\n" \
  "buffer_load_dwordx2 v[174:175], v18, s[44:47], s59 offen\n" \
  "s_add_u32 s59, s76, 0x88000\n" \
  "buffer_load_dwordx2 v[176:177], v18, s[44:47], s59 offen\n" \
  "s_add_u32 s59, s76, 0x8a000\n" \
  "buffer_load_dwordx2 v[178:179], v18, s[44:47], s59 offen\n" \
  "s_add_u32 s59, s76, 0x8c000\n" \
  "buffer_load_dwordx2 v[180:181], v18, s[44:47], s59 offen\n" \
  "s_add_u32 s59, s76, 0x8e000\n" \
  "buffer_load_dwordx2 v[182:183], v18, s[44:47], s59 offen\n" \
  "s_add_u32 s59, s76, 0x98000\n" \
  "buffer_load_dwordx2 v[184:185], v18, s[44:47], s59 offen\n" \
  "s_add_u32 s59, s76, 0x9a000\n" \
  "buffer_load_dwordx2 v[186:187], v18, s[44:47], s59 offen\n" \
  "s_add_u32 s59, s76, 0x9c000\n" \
  "buffer_load_dwordx2 v[188:189], v18, s[44:47], s59 offen\n" \
  "s_add_u32 s59, s76, 0x9e000\n" \
  "buffer_load_dwordx2 v[190:191], v18, s[44:47], s59 offen\n" \
  "s_waitcnt vmcnt(62)\n" \
  "v_perm_b32 v20, v194, v192, s68\n" \
  "v_perm_b32 v21, v194, v192, s69\n" \
  "v_perm_b32 v22, v195, v193, s68\n" \
  "v_perm_b32 v23, v195, v193, s69\n" \
  "v_accvgpr_write_b32 a16, v20\n" \
  "v_accvgpr_write_b32 a80, v21\n" \
  "v_accvgpr_write_b32 a144, v22\n" \
  "v_accvgpr_write_b32 a208, v23\n" \
  "s_waitcnt vmcnt(60)\n" \
  "v_perm_b32 v20, v198, v196, s68\n" \
  "v_perm_b32 v21, v198, v196, s69\n" \
  "v_perm_b32 v22, v199, v197, s68\n" \
  "v_perm_b32 v23, v199, v197, s69\n" \
  "v_accvgpr_write_b32 a17, v20\n" \
  "v_accvgpr_write_b32 a81, v21\n" \
  "v_accvgpr_write_b32 a145, v22\n" \
  "v_accvgpr_write_b32 a209, v23\n" \
  "s_waitcnt vmcnt(58)\n" \
  "v_perm_b32 v20, v202, v200, s68\n" \
  "v_perm_b32 v21, v202, v200, s69\n" \
  "v_perm_b32 v22, v203, v201, s68\n" \
  "v_perm_b32 v23, v203, v201, s69\n" \
  "v_accvgpr_write_b32 a18, v20\n" \
  "v_accvgpr_write_b32 a82, v21\n" \
  "v_accvgpr_write_b32 a146, v22\n" \
  "v_accvgpr_write_b32 a210, v23\n" \
  "s_waitcnt vmcnt(56)\n" \
  "v_perm_b32 v20, v206, v204, s68\n" \
  "v_perm_b32 v21, v206, v204, s69\n" \
  "v_perm_b32 v22, v207, v205, s68\n" \
  "v_perm_b32 v23, v207, v205, s69\n" \
  "v_accvgpr_write_b32 a19, v20\n" \
  "v_accvgpr_write_b32 a83, v21\n" \
  "v_accvgpr_write_b32 a147, v22\n" \
  "v_accvgpr_write_b32 a211, v23\n" \
  "s_waitcnt vmcnt(54)\n" \
  "v_perm_b32 v20, v210, v208, s68\n" \
  "v_perm_b32 v21, v210, v208, s69\n" \
  "v_perm_b32 v22, v211, v209, s68\n" \
  "v_perm_b32 v23, v211, v209, s69\n" \
  "v_accvgpr_write_b32 a20, v20\n" \
  "v_accvgpr_write_b32 a84, v21\n" \
  "v_accvgpr_write_b32 a148, v22\n" \
  "v_accvgpr_write_b32 a212, v23\n" \
  "s_waitcnt vmcnt(52)\n" \
  "v_perm_b32 v20, v214, v212, s68\n" \
  "v_perm_b32 v21, v214, v212, s69\n" \
  "v_perm_b32 v22, v215, v213, s68\n" \
  "v_perm_b32 v23, v215, v213, s69\n" \
  "v_accvgpr_write_b32 a21, v20\n" \
  "v_accvgpr_write_b32 a85, v21\n" \
  "v_accvgpr_write_b32 a149, v22\n" \
  "v_accvgpr_write_b32 a213, v23\n" \
  "s_waitcnt vmcnt(50)\n" \
  "v_perm_b32 v20, v218, v216, s68\n" \
  "v_perm_b32 v21, v218, v216, s69\n" \
  "v_perm_b32 v22, v219, v217, s68\n" \
  "v_perm_b32 v23, v219, v217, s69\n" \
  "v_accvgpr_write_b32 a22, v20\n" \
  "v_accvgpr_write_b32 a86, v21\n" \
  "v_accvgpr_write_b32 a150, v22\n" \
  "v_accvgpr_write_b32 a214, v23\n" \
  "s_waitcnt vmcnt(48)\n" \
  "v_perm_b32 v20, v222, v220, s68\n" \
  "v_perm_b32 v21, v222, v220, s69\n" \
  "v_perm_b32 v22, v223, v221, s68\n" \
  "v_perm_b32 v23, v223, v221, s69\n" \
  "v_accvgpr_write_b32 a23, v20\n" \
  "v_accvgpr_write_b32 a87, v21\n" \
  "v_accvgpr_write_b32 a151, v22\n" \
  "v_accvgpr_write_b32 a215, v23\n" \
  "s_add_u32 s59, s77, 0x0\n" \
  "buffer_load_dwordx2 v[192:193], v18, s[44:47], s59 offen\n" \
  "s_add_u32 s59, s77, 0x2000\n" \
  "buffer_load_dwordx2 v[194:195], v18, s[44:47], s59 offen\n" \
  "s_add_u32 s59, s77, 0x4000\n" \
  "buffer_load_dwordx2 v[196:197], v18, s[44:47], s59 offen\n" \
  "s_add_u32 s59, s77, 0x6000\n" \
  "buffer_load_dwordx2 v[198:199], v18, s[44:47], s59 offen\n" \
  "s_add_u32 s59, s77, 0x10000\n" \
  "buffer_load_dwordx2 v[200:201], v18, s[44:47], s59 offen\n" \
  "s_add_u32 s59, s77, 0x12000\n" \
  "buffer_load_dwordx2 v[202:203], v18, s[44:47], s59 offen\n" \
  "s_add_u32 s59, s77, 0x14000\n" \
  "buffer_load_dwordx2 v[204:205], v18, s[44:47], s59 offen\n" \
  "s_add_u32 s59, s77, 0x16000\n" \
  "buffer_load_dwordx2 v[206:207], v18, s[44:47], s59 offen\n" \
  "s_add_u32 s59, s77, 0x80000\n" \
  "buffer_load_dwordx2 v[208:209], v18, s[44:47], s59 offen\n" \
  "s_add_u32 s59, s77, 0x82000\n" \
  "buffer_load_dwordx2 v[210:211], v18, s[44:47], s59 offen\n" \
  "s_add_u32 s59, s77, 0x84000\n" \
  "buffer_load_dwordx2 v[212:213], v18, s[44:47], s59 offen\n" \
  "s_add_u32 s59, s77, 0x86000\n" \
  "buffer_load_dwordx2 v[214:215], v18, s[44:47], s59 offen\n" \
  "s_add_u32 s59, s77, 0x90000\n" \
  "buffer_load_dwordx2 v[216:217], v18, s[44:47], s59 offen\n" \
  "s_add_u32 s59, s77, 0x92000\n" \
  "buffer_load_dwordx2 v[218:219], v18, s[44:47], s59 offen\n" \
  "s_add_u32 s59, s77, 0x94000\n" \
  "buffer_load_dwordx2 v[220:221], v18, s[44:47], s59 offen\n" \
  "s_add_u32 s59, s77, 0x96000\n" \
  "buffer_load_dwordx2 v[222:223], v18, s[44:47], s59 offen\n" \
  "s_waitcnt vmcnt(62)\n" \
  "v_perm_b32 v20, v226, v224, s68\n" \
  "v_perm_b32 v21, v226, v224, s69\n" \
  "v_perm_b32 v22, v227, v225, s68\n" \
  "v_perm_b32 v23, v227, v225, s69\n" \
  "v_accvgpr_write_b32 a24, v20\n" \
  "v_accvgpr_write_b32 a88, v21\n" \
  "v_accvgpr_write_b32 a152, v22\n" \
  "v_accvgpr_write_b32 a216, v23\n" \
  "s_waitcnt vmcnt(60)\n" \
  "v_perm_b32 v20, v230, v228, s68\n" \
  "v_perm_b32 v21, v230, v228, s69\n" \
  "v_perm_b32 v22, v231, v229, s68\n" \
  "v_perm_b32 v23, v231, v229, s69\n" \
  "v_accvgpr_write_b32 a25, v20\n" \
  "v_accvgpr_write_b32 a89, v21\n" \
  "v_accvgpr_write_b32 a153, v22\n" \
  "v_accvgpr_write_b32 a217, v23\n" \
  "s_waitcnt vmcnt(58)\n" \
  "v_perm_b32 v20, v234, v232, s68\n" \
  "v_perm_b32 v21, v234, v232, s69\n" \
  "v_perm_b32 v22, v235, v233, s68\n" \
  "v_perm_b32 v23, v235, v233, s69\n" \
  "v_accvgpr_write_b32 a26, v20\n" \
  "v_accvgpr_write_b32 a90, v21\n" \
  "v_accvgpr_write_b32 a154, v22\n" \
  "v_accvgpr_write_b32 a218, v23\n" \
  "s_waitcnt vmcnt(56)\n" \
  "v_perm_b32 v20, v238, v236, s68\n" \
  "v_perm_b32 v21, v238, v236, s69\n" \
  "v_perm_b32 v22, v239, v237, s68\n" \
  "v_perm_b32 v23, v239, v237, s69\n" \
  "v_accvgpr_write_b32 a27, v20\n" \
  "v_accvgpr_write_b32 a91, v21\n" \
  "v_accvgpr_write_b32 a155, v22\n" \
  "v_accvgpr_write_b32 a219, v23\n" \
  "s_waitcnt vmcnt(54)\n" \
  "v_perm_b32 v20, v242, v240, s68\n" \
  "v_perm_b32 v21, v242, v240, s69\n" \
  "v_perm_b32 v22, v243, v241, s68\n" \
  "v_perm_b32 v23, v243, v241, s69\n" \
  "v_accvgpr_write_b32 a28, v20\n" \
  "v_accvgpr_write_b32 a92, v21\n" \
  "v_accvgpr_write_b32 a156, v22\n" \
  "v_accvgpr_write_b32 a220, v23\n" \
  "s_waitcnt vmcnt(52)\n" \
  "v_perm_b32 v20, v246, v244, s68\n" \
  "v_perm_b32 v21, v246, v244, s69\n" \
  "v_perm_b32 v22, v247, v245, s68\n" \
  "v_perm_b32 v23, v247, v245, s69\n" \
  "v_accvgpr_write_b32 a29, v20\n" \
  "v_accvgpr_write_b32 a93, v21\n" \
  "v_accvgpr_write_b32 a157, v22\n" \
  "v_accvgpr_write_b32 a221, v23\n" \
  "s_waitcnt vmcnt(50)\n" \
  "v_perm_b32 v20, v250, v248, s68\n" \
  "v_perm_b32 v21, v250, v248, s69\n" \
  "v_perm_b32 v22, v251, v249, s68\n" \
  "v_perm_b32 v23, v251, v249, s69\n" \
  "v_accvgpr_write_b32 a30, v20\n" \
  "v_accvgpr_write_b32 a94, v21\n" \
  "v_accvgpr_write_b32 a158, v22\n" \
  "v_accvgpr_write_b32 a222, v23\n" \
  "s_waitcnt vmcnt(48)\n" \
  "v_perm_b32 v20, v254, v252, s68\n" \
  "v_perm_b32 v21, v254, v252, s69\n" \
  "v_perm_b32 v22, v255, v253, s68\n" \
  "v_perm_b32 v23, v255, v253, s69\n" \
  "v_accvgpr_write_b32 a31, v20\n" \
  "v_accvgpr_write_b32 a95, v21\n" \
  "v_accvgpr_write_b32 a159, v22\n" \
  "v_accvgpr_write_b32 a223, v23\n" \
  "s_add_u32 s59, s77, 0x8000\n" \
  "buffer_load_dwordx2 v[224:225], v18, s[44:47], s59 offen\n" \
  "s_add_u32 s59, s77, 0xa000\n" \
  "buffer_load_dwordx2 v[226:227], v18, s[44:47], s59 offen\n" \
  "s_add_u32 s59, s77, 0xc000\n" \
  "buffer_load_dwordx2 v[228:229], v18, s[44:47], s59 offen\n" \
  "s_add_u32 s59, s77, 0xe000\n" \
  "buffer_load_dwordx2 v[230:231], v18, s[44:47], s59 offen\n" \
  "s_add_u32 s59, s77, 0x18000\n" \
  "buffer_load_dwordx2 v[232:233], v18, s[44:47], s59 offen\n" \
  "s_add_u32 s59, s77, 0x1a000\n" \
  "buffer_load_dwordx2 v[234:235], v18, s[44:47], s59 offen\n" \
  "s_add_u32 s59, s77, 0x1c000\n" \
  "buffer_load_dwordx2 v[236:237], v18, s[44:47], s59 offen\n" \
  "s_add_u32 s59, s77, 0x1e000\n" \
  "buffer_load_dwordx2 v[238:239], v18, s[44:47], s59 offen\n" \
  "s_add_u32 s59, s77, 0x88000\n" \
  "buffer_load_dwordx2 v[240:241], v18, s[44:47], s59 offen\n" \
  "s_add_u32 s59, s77, 0x8a000\n" \
  "buffer_load_dwordx2 v[242:243], v18, s[44:47], s59 offen\n" \
  "s_add_u32 s59, s77, 0x8c000\n" \
  "buffer_load_dwordx2 v[244:245], v18, s[44:47], s59 offen\n" \
  "s_add_u32 s59, s77, 0x8e000\n" \
  "buffer_load_dwordx2 v[246:247], v18, s[44:47], s59 offen\n" \
  "s_add_u32 s59, s77, 0x98000\n" \
  "buffer_load_dwordx2 v[248:249], v18, s[44:47], s59 offen\n" \
  "s_add_u32 s59, s77, 0x9a000\n" \
  "buffer_load_dwordx2 v[250:251], v18, s[44:47], s59 offen\n" \
  "s_add_u32 s59, s77, 0x9c000\n" \
  "buffer_load_dwordx2 v[252:253], v18, s[44:47], s59 offen\n" \
  "s_add_u32 s59, s77, 0x9e000\n" \
  "buffer_load_dwordx2 v[254:255], v18, s[44:47], s59 offen\n" \
  "s_mov_b32 s44, %[wolo]\n" \
  "s_mov_b32 s45, %[wohi]\n" \
  "s_mov_b32 s46, 0x400000\n" \
  "s_mov_b32 s47, 0x00020000\n" \
  "s_mov_b32 s59, 0x0\n" \
  "buffer_load_dwordx4 v[32:35], v17, s[44:47], s59 offen\n" \
  "s_mov_b32 s59, 0x400\n" \
  "buffer_load_dwordx4 v[36:39], v17, s[44:47], s59 offen\n" \
  "s_mov_b32 s59, 0x800\n" \
  "buffer_load_dwordx4 v[40:43], v17, s[44:47], s59 offen\n" \
  "s_mov_b32 s59, 0xc00\n" \
  "buffer_load_dwordx4 v[44:47], v17, s[44:47], s59 offen\n" \
  "s_mov_b32 s59, 0x1000\n" \
  "buffer_load_dwordx4 v[48:51], v17, s[44:47], s59 offen\n" \
  "s_mov_b32 s59, 0x1400\n" \
  "buffer_load_dwordx4 v[52:55], v17, s[44:47], s59 offen\n" \
  "s_mov_b32 s59, 0x1800\n" \
  "buffer_load_dwordx4 v[56:59], v17, s[44:47], s59 offen\n" \
  "s_mov_b32 s59, 0x1c00\n" \
  "buffer_load_dwordx4 v[60:63], v17, s[44:47], s59 offen\n" \
  "s_waitcnt vmcnt(63)\n" \
  "v_perm_b32 v20, v130, v128, s68\n" \
  "v_perm_b32 v21, v130, v128, s69\n" \
  "v_perm_b32 v22, v131, v129, s68\n" \
  "v_perm_b32 v23, v131, v129, s69\n" \
  "v_accvgpr_write_b32 a32, v20\n" \
  "v_accvgpr_write_b32 a96, v21\n" \
  "v_accvgpr_write_b32 a160, v22\n" \
  "v_accvgpr_write_b32 a224, v23\n" \
  "v_perm_b32 v20, v134, v132, s68\n" \
  "v_perm_b32 v21, v134, v132, s69\n" \
  "v_perm_b32 v22, v135, v133, s68\n" \
  "v_perm_b32 v23, v135, v133, s69\n" \
  "v_accvgpr_write_b32 a33, v20\n" \
  "v_accvgpr_write_b32 a97, v21\n" \
  "v_accvgpr_write_b32 a161, v22\n" \
  "v_accvgpr_write_b32 a225, v23\n" \
  "v_perm_b32 v20, v138, v136, s68\n" \
  "v_perm_b32 v21, v138, v136, s69\n" \
  "v_perm_b32 v22, v139, v137, s68\n" \
  "v_perm_b32 v23, v139, v137, s69\n" \
  "v_accvgpr_write_b32 a34, v20\n" \
  "v_accvgpr_write_b32 a98, v21\n" \
  "v_accvgpr_write_b32 a162, v22\n" \
  "v_accvgpr_write_b32 a226, v23\n" \
  "v_perm_b32 v20, v142, v140, s68\n" \
  "v_perm_b32 v21, v142, v140, s69\n" \
  "v_perm_b32 v22, v143, v141, s68\n" \
  "v_perm_b32 v23, v143, v141, s69\n" \
  "v_accvgpr_write_b32 a35, v20\n" \
  "v_accvgpr_write_b32 a99, v21\n" \
  "v_accvgpr_write_b32 a163, v22\n" \
  "v_accvgpr_write_b32 a227, v23\n" \
  "s_waitcnt vmcnt(62)\n" \
  "v_perm_b32 v20, v146, v144, s68\n" \
  "v_perm_b32 v21, v146, v144, s69\n" \
  "v_perm_b32 v22, v147, v145, s68\n" \
  "v_perm_b32 v23, v147, v145, s69\n" \
  "v_accvgpr_write_b32 a36, v20\n" \
  "v_accvgpr_write_b32 a100, v21\n" \
  "v_accvgpr_write_b32 a164, v22\n" \
  "v_accvgpr_write_b32 a228, v23\n" \
  "s_waitcnt vmcnt(60)\n" \
  "v_perm_b32 v20, v150, v148, s68\n" \
  "v_perm_b32 v21, v150, v148, s69\n" \
  "v_perm_b32 v22, v151, v149, s68\n" \
  "v_perm_b32 v23, v151, v149, s69\n" \
  "v_accvgpr_write_b32 a37, v20\n" \
  "v_accvgpr_write_b32 a101, v21\n" \
  "v_accvgpr_write_b32 a165, v22\n" \
  "v_accvgpr_write_b32 a229, v23\n" \
  "s_waitcnt vmcnt(58)\n" \
  "v_perm_b32 v20, v154, v152, s68\n" \
  "v_perm_b32 v21, v154, v152, s69\n" \
  "v_perm_b32 v22, v155, v153, s68\n" \
  "v_perm_b32 v23, v155, v153, s69\n" \
  "v_accvgpr_write_b32 a38, v20\n" \
  "v_accvgpr_write_b32 a102, v21\n" \
  "v_accvgpr_write_b32 a166, v22\n" \
  "v_accvgpr_write_b32 a230, v23\n" \
  "s_waitcnt vmcnt(56)\n" \
  "v_perm_b32 v20, v158, v156, s68\n" \
  "v_perm_b32 v21, v158, v156, s69\n" \
  "v_perm_b32 v22, v159, v157, s68\n" \
  "v_perm_b32 v23, v159, v157, s69\n" \
  "v_accvgpr_write_b32 a39, v20\n" \
  "v_accvgpr_write_b32 a103, v21\n" \
  "v_accvgpr_write_b32 a167, v22\n" \
  "v_accvgpr_write_b32 a231, v23\n" \
  "s_mov_b32 s59, 0x8000\n" \
  "buffer_load_dwordx4 v[64:67], v17, s[44:47], s59 offen\n" \
  "s_mov_b32 s59, 0x8400\n" \
  "buffer_load_dwordx4 v[68:71], v17, s[44:47], s59 offen\n" \
  "s_mov_b32 s59, 0x8800\n" \
  "buffer_load_dwordx4 v[72:75], v17, s[44:47], s59 offen\n" \
  "s_mov_b32 s59, 0x8c00\n" \
  "buffer_load_dwordx4 v[76:79], v17, s[44:47], s59 offen\n" \
  "s_mov_b32 s59, 0x9000\n" \
  "buffer_load_dwordx4 v[80:83], v17, s[44:47], s59 offen\n" \
  "s_mov_b32 s59, 0x9400\n" \
  "buffer_load_dwordx4 v[84:87], v17, s[44:47], s59 offen\n" \
  "s_mov_b32 s59, 0x9800\n" \
  "buffer_load_dwordx4 v[88:91], v17, s[44:47], s59 offen\n" \
  "s_mov_b32 s59, 0x9c00\n" \
  "buffer_load_dwordx4 v[92:95], v17, s[44:47], s59 offen\n" \
  "s_waitcnt vmcnt(62)\n" \
  "v_perm_b32 v20, v162, v160, s68\n" \
  "v_perm_b32 v21, v162, v160, s69\n" \
  "v_perm_b32 v22, v163, v161, s68\n" \
  "v_perm_b32 v23, v163, v161, s69\n" \
  "v_accvgpr_write_b32 a40, v20\n" \
  "v_accvgpr_write_b32 a104, v21\n" \
  "v_accvgpr_write_b32 a168, v22\n" \
  "v_accvgpr_write_b32 a232, v23\n" \
  "s_waitcnt vmcnt(60)\n" \
  "v_perm_b32 v20, v166, v164, s68\n" \
  "v_perm_b32 v21, v166, v164, s69\n" \
  "v_perm_b32 v22, v167, v165, s68\n" \
  "v_perm_b32 v23, v167, v165, s69\n" \
  "v_accvgpr_write_b32 a41, v20\n" \
  "v_accvgpr_write_b32 a105, v21\n" \
  "v_accvgpr_write_b32 a169, v22\n" \
  "v_accvgpr_write_b32 a233, v23\n" \
  "s_waitcnt vmcnt(58)\n" \
  "v_perm_b32 v20, v170, v168, s68\n" \
  "v_perm_b32 v21, v170, v168, s69\n" \
  "v_perm_b32 v22, v171, v169, s68\n" \
  "v_perm_b32 v23, v171, v169, s69\n" \
  "v_accvgpr_write_b32 a42, v20\n" \
  "v_accvgpr_write_b32 a106, v21\n" \
  "v_accvgpr_write_b32 a170, v22\n" \
  "v_accvgpr_write_b32 a234, v23\n" \
  "s_waitcnt vmcnt(56)\n" \
  "v_perm_b32 v20, v174, v172, s68\n" \
  "v_perm_b32 v21, v174, v172, s69\n" \
  "v_perm_b32 v22, v175, v173, s68\n" \
  "v_perm_b32 v23, v175, v173, s69\n" \
  "v_accvgpr_write_b32 a43, v20\n" \
  "v_accvgpr_write_b32 a107, v21\n" \
  "v_accvgpr_write_b32 a171, v22\n" \
  "v_accvgpr_write_b32 a235, v23\n" \
  "s_waitcnt vmcnt(54)\n" \
  "v_perm_b32 v20, v178, v176, s68\n" \
  "v_perm_b32 v21, v178, v176, s69\n" \
  "v_perm_b32 v22, v179, v177, s68\n" \
  "v_perm_b32 v23, v179, v177, s69\n" \
  "v_accvgpr_write_b32 a44, v20\n" \
  "v_accvgpr_write_b32 a108, v21\n" \
  "v_accvgpr_write_b32 a172, v22\n" \
  "v_accvgpr_write_b32 a236, v23\n" \
  "s_waitcnt vmcnt(52)\n" \
  "v_perm_b32 v20, v182, v180, s68\n" \
  "v_perm_b32 v21, v182, v180, s69\n" \
  "v_perm_b32 v22, v183, v181, s68\n" \
  "v_perm_b32 v23, v183, v181, s69\n" \
  "v_accvgpr_write_b32 a45, v20\n" \
  "v_accvgpr_write_b32 a109, v21\n" \
  "v_accvgpr_write_b32 a173, v22\n" \
  "v_accvgpr_write_b32 a237, v23\n" \
  "s_waitcnt vmcnt(50)\n" \
  "v_perm_b32 v20, v186, v184, s68\n" \
  "v_perm_b32 v21, v186, v184, s69\n" \
  "v_perm_b32 v22, v187, v185, s68\n" \
  "v_perm_b32 v23, v187, v185, s69\n" \
  "v_accvgpr_write_b32 a46, v20\n" \
  "v_accvgpr_write_b32 a110, v21\n" \
  "v_accvgpr_write_b32 a174, v22\n" \
  "v_accvgpr_write_b32 a238, v23\n" \
  "s_waitcnt vmcnt(48)\n" \
  "v_perm_b32 v20, v190, v188, s68\n" \
  "v_perm_b32 v21, v190, v188, s69\n" \
  "v_perm_b32 v22, v191, v189, s68\n" \
  "v_perm_b32 v23, v191, v189, s69\n" \
  "v_accvgpr_write_b32 a47, v20\n" \
  "v_accvgpr_write_b32 a111, v21\n" \
  "v_accvgpr_write_b32 a175, v22\n" \
  "v_accvgpr_write_b32 a239, v23\n" \
  "s_mov_b32 s59, 0x10000\n" \
  "buffer_load_dwordx4 v[96:99], v17, s[44:47], s59 offen\n" \
  "s_mov_b32 s59, 0x10400\n" \
  "buffer_load_dwordx4 v[100:103], v17, s[44:47], s59 offen\n" \
  "s_mov_b32 s59, 0x10800\n" \
  "buffer_load_dwordx4 v[104:107], v17, s[44:47], s59 offen\n" \
  "s_mov_b32 s59, 0x10c00\n" \
  "buffer_load_dwordx4 v[108:111], v17, s[44:47], s59 offen\n" \
  "s_mov_b32 s59, 0x11000\n" \
  "buffer_load_dwordx4 v[112:115], v17, s[44:47], s59 offen\n" \
  "s_mov_b32 s59, 0x11400\n" \
  "buffer_load_dwordx4 v[116:119], v17, s[44:47], s59 offen\n" \
  "s_mov_b32 s59, 0x11800\n" \
  "buffer_load_dwordx4 v[120:123], v17, s[44:47], s59 offen\n" \
  "s_mov_b32 s59, 0x11c00\n" \
  "buffer_load_dwordx4 v[124:127], v17, s[44:47], s59 offen\n" \
  "s_waitcnt vmcnt(54)\n" \
  "v_perm_b32 v20, v194, v192, s68\n" \
  "v_perm_b32 v21, v194, v192, s69\n" \
  "v_perm_b32 v22, v195, v193, s68\n" \
  "v_perm_b32 v23, v195, v193, s69\n" \
  "v_accvgpr_write_b32 a48, v20\n" \
  "v_accvgpr_write_b32 a112, v21\n" \
  "v_accvgpr_write_b32 a176, v22\n" \
  "v_accvgpr_write_b32 a240, v23\n" \
  "s_waitcnt vmcnt(52)\n" \
  "v_perm_b32 v20, v198, v196, s68\n" \
  "v_perm_b32 v21, v198, v196, s69\n" \
  "v_perm_b32 v22, v199, v197, s68\n" \
  "v_perm_b32 v23, v199, v197, s69\n" \
  "v_accvgpr_write_b32 a49, v20\n" \
  "v_accvgpr_write_b32 a113, v21\n" \
  "v_accvgpr_write_b32 a177, v22\n" \
  "v_accvgpr_write_b32 a241, v23\n" \
  "s_waitcnt vmcnt(50)\n" \
  "v_perm_b32 v20, v202, v200, s68\n" \
  "v_perm_b32 v21, v202, v200, s69\n" \
  "v_perm_b32 v22, v203, v201, s68\n" \
  "v_perm_b32 v23, v203, v201, s69\n" \
  "v_accvgpr_write_b32 a50, v20\n" \
  "v_accvgpr_write_b32 a114, v21\n" \
  "v_accvgpr_write_b32 a178, v22\n" \
  "v_accvgpr_write_b32 a242, v23\n" \
  "s_waitcnt vmcnt(48)\n" \
  "v_perm_b32 v20, v206, v204, s68\n" \
  "v_perm_b32 v21, v206, v204, s69\n" \
  "v_perm_b32 v22, v207, v205, s68\n" \
  "v_perm_b32 v23, v207, v205, s69\n" \
  "v_accvgpr_write_b32 a51, v20\n" \
  "v_accvgpr_write_b32 a115, v21\n" \
  "v_accvgpr_write_b32 a179, v22\n" \
  "v_accvgpr_write_b32 a243, v23\n" \
  "s_waitcnt vmcnt(46)\n" \
  "v_perm_b32 v20, v210, v208, s68\n" \
  "v_perm_b32 v21, v210, v208, s69\n" \
  "v_perm_b32 v22, v211, v209, s68\n" \
  "v_perm_b32 v23, v211, v209, s69\n" \
  "v_accvgpr_write_b32 a52, v20\n" \
  "v_accvgpr_write_b32 a116, v21\n" \
  "v_accvgpr_write_b32 a180, v22\n" \
  "v_accvgpr_write_b32 a244, v23\n" \
  "s_waitcnt vmcnt(44)\n" \
  "v_perm_b32 v20, v214, v212, s68\n" \
  "v_perm_b32 v21, v214, v212, s69\n" \
  "v_perm_b32 v22, v215, v213, s68\n" \
  "v_perm_b32 v23, v215, v213, s69\n" \
  "v_accvgpr_write_b32 a53, v20\n" \
  "v_accvgpr_write_b32 a117, v21\n" \
  "v_accvgpr_write_b32 a181, v22\n" \
  "v_accvgpr_write_b32 a245, v23\n" \
  "s_waitcnt vmcnt(42)\n" \
  "v_perm_b32 v20, v218, v216, s68\n" \
  "v_perm_b32 v21, v218, v216, s69\n" \
  "v_perm_b32 v22, v219, v217, s68\n" \
  "v_perm_b32 v23, v219, v217, s69\n" \
  "v_accvgpr_write_b32 a54, v20\n" \
  "v_accvgpr_write_b32 a118, v21\n" \
  "v_accvgpr_write_b32 a182, v22\n" \
  "v_accvgpr_write_b32 a246, v23\n" \
  "s_waitcnt vmcnt(40)\n" \
  "v_perm_b32 v20, v222, v220, s68\n" \
  "v_perm_b32 v21, v222, v220, s69\n" \
  "v_perm_b32 v22, v223, v221, s68\n" \
  "v_perm_b32 v23, v223, v221, s69\n" \
  "v_accvgpr_write_b32 a55, v20\n" \
  "v_accvgpr_write_b32 a119, v21\n" \
  "v_accvgpr_write_b32 a183, v22\n" \
  "v_accvgpr_write_b32 a247, v23\n" \
  "s_waitcnt vmcnt(38)\n" \
  "v_perm_b32 v20, v226, v224, s68\n" \
  "v_perm_b32 v21, v226, v224, s69\n" \
  "v_perm_b32 v22, v227, v225, s68\n" \
  "v_perm_b32 v23, v227, v225, s69\n" \
  "v_accvgpr_write_b32 a56, v20\n" \
  "v_accvgpr_write_b32 a120, v21\n" \
  "v_accvgpr_write_b32 a184, v22\n" \
  "v_accvgpr_write_b32 a248, v23\n" \
  "s_waitcnt vmcnt(36)\n" \
  "v_perm_b32 v20, v230, v228, s68\n" \
  "v_perm_b32 v21, v230, v228, s69\n" \
  "v_perm_b32 v22, v231, v229, s68\n" \
  "v_perm_b32 v23, v231, v229, s69\n" \
  "v_accvgpr_write_b32 a57, v20\n" \
  "v_accvgpr_write_b32 a121, v21\n" \
  "v_accvgpr_write_b32 a185, v22\n" \
  "v_accvgpr_write_b32 a249, v23\n" \
  "s_waitcnt vmcnt(34)\n" \
  "v_perm_b32 v20, v234, v232, s68\n" \
  "v_perm_b32 v21, v234, v232, s69\n" \
  "v_perm_b32 v22, v235, v233, s68\n" \
  "v_perm_b32 v23, v235, v233, s69\n" \
  "v_accvgpr_write_b32 a58, v20\n" \
  "v_accvgpr_write_b32 a122, v21\n" \
  "v_accvgpr_write_b32 a186, v22\n" \
  "v_accvgpr_write_b32 a250, v23\n" \
  "s_waitcnt vmcnt(32)\n" \
  "v_perm_b32 v20, v238, v236, s68\n" \
  "v_perm_b32 v21, v238, v236, s69\n" \
  "v_perm_b32 v22, v239, v237, s68\n" \
  "v_perm_b32 v23, v239, v237, s69\n" \
  "v_accvgpr_write_b32 a59, v20\n" \
  "v_accvgpr_write_b32 a123, v21\n" \
  "v_accvgpr_write_b32 a187, v22\n" \
  "v_accvgpr_write_b32 a251, v23\n" \
  "s_waitcnt vmcnt(30)\n" \
  "v_perm_b32 v20, v242, v240, s68\n" \
  "v_perm_b32 v21, v242, v240, s69\n" \
  "v_perm_b32 v22, v243, v241, s68\n" \
  "v_perm_b32 v23, v243, v241, s69\n" \
  "v_accvgpr_write_b32 a60, v20\n" \
  "v_accvgpr_write_b32 a124, v21\n" \
  "v_accvgpr_write_b32 a188, v22\n" \
  "v_accvgpr_write_b32 a252, v23\n" \
  "s_waitcnt vmcnt(28)\n" \
  "v_perm_b32 v20, v246, v244, s68\n" \
  "v_perm_b32 v21, v246, v244, s69\n" \
  "v_perm_b32 v22, v247, v245, s68\n" \
  "v_perm_b32 v23, v247, v245, s69\n" \
  "v_accvgpr_write_b32 a61, v20\n" \
  "v_accvgpr_write_b32 a125, v21\n" \
  "v_accvgpr_write_b32 a189, v22\n" \
  "v_accvgpr_write_b32 a253, v23\n" \
  "s_waitcnt vmcnt(26)\n" \
  "v_perm_b32 v20, v250, v248, s68\n" \
  "v_perm_b32 v21, v250, v248, s69\n" \
  "v_perm_b32 v22, v251, v249, s68\n" \
  "v_perm_b32 v23, v251, v249, s69\n" \
  "v_accvgpr_write_b32 a62, v20\n" \
  "v_accvgpr_write_b32 a126, v21\n" \
  "v_accvgpr_write_b32 a190, v22\n" \
  "v_accvgpr_write_b32 a254, v23\n" \
  "s_waitcnt vmcnt(24)\n" \
  "v_perm_b32 v20, v254, v252, s68\n" \
  "v_perm_b32 v21, v254, v252, s69\n" \
  "v_perm_b32 v22, v255, v253, s68\n" \
  "v_perm_b32 v23, v255, v253, s69\n" \
  "v_accvgpr_write_b32 a63, v20\n" \
  "v_accvgpr_write_b32 a127, v21\n" \
  "v_accvgpr_write_b32 a191, v22\n" \
  "v_accvgpr_write_b32 a255, v23\n" \
  "s_waitcnt vmcnt(22)\n" \
  "v_perm_b32 v128, v36, v32, s68\n" \
  "s_waitcnt vmcnt(20)\n" \
  "v_perm_b32 v129, v44, v40, s68\n" \
  "s_waitcnt vmcnt(18)\n" \
  "v_perm_b32 v130, v52, v48, s68\n" \
  "s_waitcnt vmcnt(16)\n" \
  "v_perm_b32 v131, v60, v56, s68\n" \
  "v_perm_b32 v144, v36, v32, s69\n" \
  "v_perm_b32 v145, v44, v40, s69\n" \
  "v_perm_b32 v146, v52, v48, s69\n" \
  "v_perm_b32 v147, v60, v56, s69\n" \
  "v_perm_b32 v160, v37, v33, s68\n" \
  "v_perm_b32 v161, v45, v41, s68\n" \
  "v_perm_b32 v162, v53, v49, s68\n" \
  "v_perm_b32 v163, v61, v57, s68\n" \
  "v_perm_b32 v176, v37, v33, s69\n" \
  "v_perm_b32 v177, v45, v41, s69\n" \
  "v_perm_b32 v178, v53, v49, s69\n" \
  "v_perm_b32 v179, v61, v57, s69\n" \
  "v_perm_b32 v192, v38, v34, s68\n" \
  "v_perm_b32 v193, v46, v42, s68\n" \
  "v_perm_b32 v194, v54, v50, s68\n" \
  "v_perm_b32 v195, v62, v58, s68\n" \
  "v_perm_b32 v208, v38, v34, s69\n" \
  "v_perm_b32 v209, v46, v42, s69\n" \
  "v_perm_b32 v210, v54, v50, s69\n" \
  "v_perm_b32 v211, v62, v58, s69\n" \
  "v_perm_b32 v224, v39, v35, s68\n" \
  "v_perm_b32 v225, v47, v43, s68\n" \
  "v_perm_b32 v226, v55, v51, s68\n" \
  "v_perm_b32 v227, v63, v59, s68\n" \
  "v_perm_b32 v240, v39, v35, s69\n" \
  "v_perm_b32 v241, v47, v43, s69\n" \
  "v_perm_b32 v242, v55, v51, s69\n" \
  "v_perm_b32 v243, v63, v59, s69\n" \
  "s_mov_b32 s59, 0x18000\n" \
  "buffer_load_dwordx4 v[32:35], v17, s[44:47], s59 offen\n" \
  "s_mov_b32 s59, 0x18400\n" \
  "buffer_load_dwordx4 v[36:39], v17, s[44:47], s59 offen\n" \
  "s_mov_b32 s59, 0x18800\n" \
  "buffer_load_dwordx4 v[40:43], v17, s[44:47], s59 offen\n" \
  "s_mov_b32 s59, 0x18c00\n" \
  "buffer_load_dwordx4 v[44:47], v17, s[44:47], s59 offen\n" \
  "s_mov_b32 s59, 0x19000\n" \
  "buffer_load_dwordx4 v[48:51], v17, s[44:47], s59 offen\n" \
  "s_mov_b32 s59, 0x19400\n" \
  "buffer_load_dwordx4 v[52:55], v17, s[44:47], s59 offen\n" \
  "s_mov_b32 s59, 0x19800\n" \
  "buffer_load_dwordx4 v[56:59], v17, s[44:47], s59 offen\n" \
  "s_mov_b32 s59, 0x19c00\n" \
  "buffer_load_dwordx4 v[60:63], v17, s[44:47], s59 offen\n" \
  "s_waitcnt vmcnt(22)\n" \
  "v_perm_b32 v132, v68, v64, s68\n" \
  "s_waitcnt vmcnt(20)\n" \
  "v_perm_b32 v133, v76, v72, s68\n" \
  "s_waitcnt vmcnt(18)\n" \
  "v_perm_b32 v134, v84, v80, s68\n" \
  "s_waitcnt vmcnt(16)\n" \
  "v_perm_b32 v135, v92, v88, s68\n" \
  "v_perm_b32 v148, v68, v64, s69\n" \
  "v_perm_b32 v149, v76, v72, s69\n" \
  "v_perm_b32 v150, v84, v80, s69\n" \
  "v_perm_b32 v151, v92, v88, s69\n" \
  "v_perm_b32 v164, v69, v65, s68\n" \
  "v_perm_b32 v165, v77, v73, s68\n" \
  "v_perm_b32 v166, v85, v81, s68\n" \
  "v_perm_b32 v167, v93, v89, s68\n" \
  "v_perm_b32 v180, v69, v65, s69\n" \
  "v_perm_b32 v181, v77, v73, s69\n" \
  "v_perm_b32 v182, v85, v81, s69\n" \
  "v_perm_b32 v183, v93, v89, s69\n" \
  "v_perm_b32 v196, v70, v66, s68\n" \
  "v_perm_b32 v197, v78, v74, s68\n" \
  "v_perm_b32 v198, v86, v82, s68\n" \
  "v_perm_b32 v199, v94, v90, s68\n" \
  "v_perm_b32 v212, v70, v66, s69\n" \
  "v_perm_b32 v213, v78, v74, s69\n" \
  "v_perm_b32 v214, v86, v82, s69\n" \
  "v_perm_b32 v215, v94, v90, s69\n" \
  "v_perm_b32 v228, v71, v67, s68\n" \
  "v_perm_b32 v229, v79, v75, s68\n" \
  "v_perm_b32 v230, v87, v83, s68\n" \
  "v_perm_b32 v231, v95, v91, s68\n" \
  "v_perm_b32 v244, v71, v67, s69\n" \
  "v_perm_b32 v245, v79, v75, s69\n" \
  "v_perm_b32 v246, v87, v83, s69\n" \
  "v_perm_b32 v247, v95, v91, s69\n" \
  "s_waitcnt vmcnt(14)\n" \
  "v_perm_b32 v136, v100, v96, s68\n" \
  "s_waitcnt vmcnt(12)\n" \
  "v_perm_b32 v137, v108, v104, s68\n" \
  "s_waitcnt vmcnt(10)\n" \
  "v_perm_b32 v138, v116, v112, s68\n" \
  "s_waitcnt vmcnt(8)\n" \
  "v_perm_b32 v139, v124, v120, s68\n" \
  "v_perm_b32 v152, v100, v96, s69\n" \
  "v_perm_b32 v153, v108, v104, s69\n" \
  "v_perm_b32 v154, v116, v112, s69\n" \
  "v_perm_b32 v155, v124, v120, s69\n" \
  "v_perm_b32 v168, v101, v97, s68\n" \
  "v_perm_b32 v169, v109, v105, s68\n" \
  "v_perm_b32 v170, v117, v113, s68\n" \
  "v_perm_b32 v171, v125, v121, s68\n" \
  "v_perm_b32 v184, v101, v97, s69\n" \
  "v_perm_b32 v185, v109, v105, s69\n" \
  "v_perm_b32 v186, v117, v113, s69\n" \
  "v_perm_b32 v187, v125, v121, s69\n" \
  "v_perm_b32 v200, v102, v98, s68\n" \
  "v_perm_b32 v201, v110, v106, s68\n" \
  "v_perm_b32 v202, v118, v114, s68\n" \
  "v_perm_b32 v203, v126, v122, s68\n" \
  "v_perm_b32 v216, v102, v98, s69\n" \
  "v_perm_b32 v217, v110, v106, s69\n" \
  "v_perm_b32 v218, v118, v114, s69\n" \
  "v_perm_b32 v219, v126, v122, s69\n" \
  "v_perm_b32 v232, v103, v99, s68\n" \
  "v_perm_b32 v233, v111, v107, s68\n" \
  "v_perm_b32 v234, v119, v115, s68\n" \
  "v_perm_b32 v235, v127, v123, s68\n" \
  "v_perm_b32 v248, v103, v99, s69\n" \
  "v_perm_b32 v249, v111, v107, s69\n" \
  "v_perm_b32 v250, v119, v115, s69\n" \
  "v_perm_b32 v251, v127, v123, s69\n" \
  "s_waitcnt vmcnt(6)\n" \
  "v_perm_b32 v140, v36, v32, s68\n" \
  "s_waitcnt vmcnt(4)\n" \
  "v_perm_b32 v141, v44, v40, s68\n" \
  "s_waitcnt vmcnt(2)\n" \
  "v_perm_b32 v142, v52, v48, s68\n" \
  "s_waitcnt vmcnt(0)\n" \
  "v_perm_b32 v143, v60, v56, s68\n" \
  "v_perm_b32 v156, v36, v32, s69\n" \
  "v_perm_b32 v157, v44, v40, s69\n" \
  "v_perm_b32 v158, v52, v48, s69\n" \
  "v_perm_b32 v159, v60, v56, s69\n" \
  "v_perm_b32 v172, v37, v33, s68\n" \
  "v_perm_b32 v173, v45, v41, s68\n" \
  "v_perm_b32 v174, v53, v49, s68\n" \
  "v_perm_b32 v175, v61, v57, s68\n" \
  "v_perm_b32 v188, v37, v33, s69\n" \
  "v_perm_b32 v189, v45, v41, s69\n" \
  "v_perm_b32 v190, v53, v49, s69\n" \
  "v_perm_b32 v191, v61, v57, s69\n" \
  "v_perm_b32 v204, v38, v34, s68\n" \
  "v_perm_b32 v205, v46, v42, s68\n" \
  "v_perm_b32 v206, v54, v50, s68\n" \
  "v_perm_b32 v207, v62, v58, s68\n" \
  "v_perm_b32 v220, v38, v34, s69\n" \
  "v_perm_b32 v221, v46, v42, s69\n" \
  "v_perm_b32 v222, v54, v50, s69\n" \
  "v_perm_b32 v223, v62, v58, s69\n" \
  "v_perm_b32 v236, v39, v35, s68\n" \
  "v_perm_b32 v237, v47, v43, s68\n" \
  "v_perm_b32 v238, v55, v51, s68\n" \
  "v_perm_b32 v239, v63, v59, s68\n" \
  "v_perm_b32 v252, v39, v35, s69\n" \
  "v_perm_b32 v253, v47, v43, s69\n" \
  "v_perm_b32 v254, v55, v51, s69\n" \
  "v_perm_b32 v255, v63, v59, s69\n" \
  "v_mov_b32 v124, 0x3f803f80\n" \
  "v_mov_b32 v125, 0x3f803f80\n" \
  "v_mov_b32 v126, 0x3f803f80\n" \
  "v_mov_b32 v127, 0x3f803f80\n" \
  "s_waitcnt vmcnt(0) lgkmcnt(0)\n" \
  "s_mov_b32 s61, 0\n" \
  "HKV0_%=:\n" \
  "buffer_load_dword v10, off, s[40:43], s58 sc1\n" \
  "s_waitcnt vmcnt(0)\n" \
  "v_readfirstlane_b32 s60, v10\n" \
  "s_nop 3 ; auto\n" \
  "s_cmp_eq_u32 s60, 4\n" \
  "s_cbranch_scc1 HKV0_OK_%=\n" \
  "s_add_u32 s61, s61, 1\n" \
  "s_cmp_gt_u32 s61, 0x40000\n" \
  "s_cbranch_scc1 HABORT_%=\n" \
  "s_sleep 4\n" \
  "s_branch HKV0_%=\n" \
  "HKV0_OK_%=:\n" \
  "s_add_u32 s60, s57, 0x3000000\n" \
  "s_add_u32 m0, s62, s65\n" \
  "s_nop 0 ; auto\n" \
  "buffer_load_dwordx4 v0, s[40:43], s60 offen sc1 lds\n" \
  "buffer_load_dwordx4 v0, s[40:43], s60 offen offset:1024 sc1 lds\n" \
  "s_add_u32 s60, s57, 0x4000000\n" \
  "s_add_u32 m0, s63, s65\n" \
  "s_nop 0 ; auto\n" \
  "buffer_load_dwordx4 v0, s[40:43], s60 offen sc1 lds\n" \
  "buffer_load_dwordx4 v0, s[40:43], s60 offen offset:1024 sc1 lds\n" \
  "s_add_u32 s59, s58, 2048\n" \
  "buffer_load_dword v10, off, s[40:43], s59 sc1\n" \
  "s_waitcnt vmcnt(0)\n" \
  "s_barrier\n" \
  "HLOOP_%=:\n" \
  "buffer_load_dwordx4 v[16:19], v0, s[40:43], s49 offen sc1\n" \
  "buffer_load_dwordx4 v[20:23], v0, s[40:43], s49 offen offset:1024 sc1\n" \
  "s_mov_b32 s61, 0\n" \
  "v_add_u32 v120, s84, v0\n" \
  "s_waitcnt vmcnt(2)\n" \
  "v_mov_b64 v[48:49], 0\n" \
  "v_mov_b64 v[50:51], 0\n" \
  "v_mov_b64 v[52:53], 0\n" \
  "v_mov_b64 v[54:55], 0\n" \
  "v_mov_b64 v[56:57], 0\n" \
  "v_mov_b64 v[58:59], 0\n" \
  "v_mov_b64 v[60:61], 0\n" \
  "v_mov_b64 v[62:63], 0\n" \
  "v_add_u32 v121, s65, v0\n" \
  "v_add_u32 v12, s65, v6\n" \
  "s_xor_b32 s65, s65, 8192\n" \
  ".rept %c[wg]\n" \
  "s_nop 7\n" \
  ".endr\n" \
  "buffer_load_dwordx4 v[96:99], v0, s[40:43], s49 offen sc1\n" \
  "buffer_load_dwordx4 v[100:103], v0, s[40:43], s49 offen offset:1024 sc1\n" \
  "s_nop 7\n" \
  ".rept %c[wg]\n" \
  "s_nop 7\n" \
  ".endr\n" \
  "buffer_load_dwordx4 v[104:107], v0, s[40:43], s49 offen sc1\n" \
  "buffer_load_dwordx4 v[108:111], v0, s[40:43], s49 offen offset:1024 sc1\n" \
  "s_waitcnt vmcnt(5)\n" \
  "v_max3_u32 v47, v16, v17, v18\n" \
  "s_waitcnt vmcnt(4)\n" \
  "v_max3_u32 v47, v47, v19, v20\n" \
  "v_max3_u32 v47, v47, v21, v22\n" \
  "v_max_u32 v47, v47, v23\n" \
  "v_cmp_eq_u32 vcc, -1, v47\n" \
  "s_cbranch_vccnz HMB_MISS_%=\n" \
  "HMB_OK_%=:\n" \
  "ds_write_b128 v120, v[16:19]\n" \
  "ds_write_b128 v120, v[20:23] offset:1024\n" \
  "v_smfmac_f32_16x16x64_bf16 v[48:51], v[16:19], a[0:7], v1\n" \
  "v_smfmac_f32_16x16x64_bf16 v[52:55], v[16:19], a[64:71], v1\n" \
  "v_smfmac_f32_16x16x64_bf16 v[56:59], v[16:19], a[128:135], v1\n" \
  "v_smfmac_f32_16x16x64_bf16 v[60:63], v[16:19], a[192:199], v1\n" \
  "s_waitcnt lgkmcnt(0)\n" \
  "s_barrier\n" \
  "ds_read_b128 v[24:27], v13\n" \
  "ds_read_b128 v[28:31], v13 offset:1024\n" \
  "ds_read_b128 v[32:35], v14\n" \
  "ds_read_b128 v[36:39], v14 offset:1024\n" \
  "ds_read_b128 v[40:43], v15\n" \
  "ds_read_b128 v[44:47], v15 offset:1024\n" \
  "v_smfmac_f32_16x16x64_bf16 v[48:51], v[20:23], a[8:15], v1\n" \
  "v_smfmac_f32_16x16x64_bf16 v[52:55], v[20:23], a[72:79], v1\n" \
  "v_smfmac_f32_16x16x64_bf16 v[56:59], v[20:23], a[136:143], v1\n" \
  "v_smfmac_f32_16x16x64_bf16 v[60:63], v[20:23], a[200:207], v1\n" \
  "s_waitcnt lgkmcnt(5)\n" \
  "v_smfmac_f32_16x16x64_bf16 v[48:51], v[24:27], a[16:23], v1\n" \
  "v_readfirstlane_b32 s71, v10\n" \
  "v_smfmac_f32_16x16x64_bf16 v[52:55], v[24:27], a[80:87], v1\n" \
  "v_smfmac_f32_16x16x64_bf16 v[56:59], v[24:27], a[144:151], v1\n" \
  "v_smfmac_f32_16x16x64_bf16 v[60:63], v[24:27], a[208:215], v1\n" \
  "s_waitcnt lgkmcnt(4)\n" \
  "v_smfmac_f32_16x16x64_bf16 v[48:51], v[28:31], a[24:31], v1\n" \
  "s_cmp_lg_u32 s71, 4\n" \
  "s_cbranch_scc1 HKVWAIT_%=\n" \
  "HKV_OK_%=:\n" \
  "s_add_u32 s60, s57, 0x3040000\n" \
  "s_add_u32 m0, s62, s65\n" \
  "v_smfmac_f32_16x16x64_bf16 v[52:55], v[28:31], a[88:95], v1\n" \
  "ds_read_b128 v[64:67], v121\n" \
  "v_smfmac_f32_16x16x64_bf16 v[56:59], v[28:31], a[152:159], v1\n" \
  "buffer_load_dwordx4 v0, s[40:43], s60 offen sc1 lds\n" \
  "v_smfmac_f32_16x16x64_bf16 v[60:63], v[28:31], a[216:223], v1\n" \
  "ds_read_b128 v[80:83], v121 offset:4096\n" \
  "s_waitcnt lgkmcnt(5)\n" \
  "v_smfmac_f32_16x16x64_bf16 v[48:51], v[32:35], a[32:39], v1\n" \
  "v_smfmac_f32_16x16x64_bf16 v[52:55], v[32:35], a[96:103], v1\n" \
  "ds_read_b128 v[68:71], v121 offset:1024\n" \
  "v_smfmac_f32_16x16x64_bf16 v[56:59], v[32:35], a[160:167], v1\n" \
  "buffer_load_dwordx4 v0, s[40:43], s60 offen offset:1024 sc1 lds\n" \
  "v_smfmac_f32_16x16x64_bf16 v[60:63], v[32:35], a[224:231], v1\n" \
  "ds_read_b128 v[84:87], v121 offset:5120\n" \
  "s_waitcnt lgkmcnt(6)\n" \
  "v_smfmac_f32_16x16x64_bf16 v[48:51], v[36:39], a[40:47], v1\n" \
  "s_add_u32 s60, s57, 0x4040000\n" \
  "s_add_u32 m0, s63, s65\n" \
  "v_smfmac_f32_16x16x64_bf16 v[52:55], v[36:39], a[104:111], v1\n" \
  "ds_read_b128 v[72:75], v121 offset:2048\n" \
  "v_smfmac_f32_16x16x64_bf16 v[56:59], v[36:39], a[168:175], v1\n" \
  "buffer_load_dwordx4 v0, s[40:43], s60 offen sc1 lds\n" \
  "v_smfmac_f32_16x16x64_bf16 v[60:63], v[36:39], a[232:239], v1\n" \
  "ds_read_b128 v[88:91], v121 offset:6144\n" \
  "s_waitcnt lgkmcnt(7)\n" \
  "v_smfmac_f32_16x16x64_bf16 v[48:51], v[40:43], a[48:55], v1\n" \
  "v_smfmac_f32_16x16x64_bf16 v[52:55], v[40:43], a[112:119], v1\n" \
  "ds_read_b128 v[76:79], v121 offset:3072\n" \
  "v_smfmac_f32_16x16x64_bf16 v[56:59], v[40:43], a[176:183], v1\n" \
  "buffer_load_dwordx4 v0, s[40:43], s60 offen offset:1024 sc1 lds\n" \
  "v_smfmac_f32_16x16x64_bf16 v[60:63], v[40:43], a[240:247], v1\n" \
  "ds_read_b128 v[92:95], v121 offset:7168\n" \
  "s_waitcnt lgkmcnt(8)\n" \
  "v_smfmac_f32_16x16x64_bf16 v[48:51], v[44:47], a[56:63], v1\n" \
  "s_add_u32 s60, s58, 4096\n" \
  "v_smfmac_f32_16x16x64_bf16 v[52:55], v[44:47], a[120:127], v1\n" \
  "v_smfmac_f32_16x16x64_bf16 v[56:59], v[44:47], a[184:191], v1\n" \
  "buffer_load_dword v10, off, s[40:43], s60 sc1\n" \
  "v_smfmac_f32_16x16x64_bf16 v[60:63], v[44:47], a[248:255], v1\n" \
  "s_and_b32 s59, s48, 1\n" \
  "s_cmp_eq_u32 s59, 0\n" \
  "s_cselect_b32 s51, s52, s53\n" \
  "s_cselect_b32 s72, s79, s73\n" \
  "s_bfe_u32 s82, s48, 0x10001\n" \
  "s_add_u32 s70, s48, 1\n" \
  "s_waitcnt vmcnt(5)\n" \
  "v_pk_add_f32 v[96:97], v[48:49], v[50:51]\n" \
  "v_pk_add_f32 v[98:99], v[52:53], v[54:55]\n" \
  "v_pk_add_f32 v[100:101], v[56:57], v[58:59]\n" \
  "v_pk_add_f32 v[102:103], v[60:61], v[62:63]\n" \
  "v_cvt_pk_bf16_f32 v104, v96, v97\n" \
  "v_cvt_pk_bf16_f32 v105, v98, v99\n" \
  "v_cvt_pk_bf16_f32 v106, v100, v101\n" \
  "v_cvt_pk_bf16_f32 v107, v102, v103\n" \
  "ds_write2st64_b32 v2, v104, v105 offset0:0 offset1:4\n" \
  "ds_write2st64_b32 v2, v106, v107 offset0:8 offset1:12\n" \
  "s_waitcnt lgkmcnt(0)\n" \
  "s_barrier\n" \
  "buffer_load_dwordx4 v[120:123], v0, s[40:43], s72 offen sc1\n" \
  "ds_read_b64_tr_b16 v[48:49], v3\n" \
  "ds_read_b64_tr_b16 v[50:51], v3 offset:1024\n" \
  "ds_read_b64_tr_b16 v[52:53], v3 offset:2048\n" \
  "ds_read_b64_tr_b16 v[54:55], v3 offset:3072\n" \
  "ds_read_b64_tr_b16 v[56:57], v4\n" \
  "ds_read_b64_tr_b16 v[58:59], v4 offset:1024\n" \
  "ds_read_b64_tr_b16 v[60:61], v4 offset:2048\n" \
  "ds_read_b64_tr_b16 v[62:63], v4 offset:3072\n" \
  "v_mov_b64 v[16:17], 0\n" \
  "v_mov_b64 v[18:19], 0\n" \
  "s_waitcnt lgkmcnt(6)\n" \
  "v_mfma_f32_16x16x32_bf16 v[96:99], v[48:51], v[48:51], 0\n" \
  "v_mfma_f32_16x16x32_bf16 v[100:103], v[64:67], v[48:51], 0\n" \
  "ds_read_b128 v[108:111], v12\n" \
  "v_mfma_f32_16x16x32_bf16 v[104:107], v[80:83], v[48:51], 0\n" \
  "ds_read_b128 v[112:115], v12 offset:1024\n" \
  "s_waitcnt lgkmcnt(6)\n" \
  "v_mfma_f32_16x16x32_bf16 v[96:99], v[52:55], v[52:55], v[96:99]\n" \
  "v_mov_b64 v[20:21], 0\n" \
  "v_mfma_f32_16x16x32_bf16 v[100:103], v[68:71], v[52:55], v[100:103]\n" \
  "v_mov_b64 v[22:23], 0\n" \
  "v_mfma_f32_16x16x32_bf16 v[104:107], v[84:87], v[52:55], v[104:107]\n" \
  "s_waitcnt lgkmcnt(4)\n" \
  "v_mfma_f32_16x16x32_bf16 v[96:99], v[56:59], v[56:59], v[96:99]\n" \
  "buffer_load_dwordx4 v[64:67], v0, s[40:43], s72 offen sc1\n" \
  "v_mfma_f32_16x16x32_bf16 v[100:103], v[72:75], v[56:59], v[100:103]\n" \
  "v_mov_b64 v[24:25], 0\n" \
  "v_mfma_f32_16x16x32_bf16 v[104:107], v[88:91], v[56:59], v[104:107]\n" \
  "v_mov_b64 v[26:27], 0\n" \
  "s_waitcnt lgkmcnt(2)\n" \
  "v_mfma_f32_16x16x32_bf16 v[96:99], v[60:63], v[60:63], v[96:99]\n" \
  "v_mov_b64 v[28:29], 0\n" \
  "v_mfma_f32_16x16x32_bf16 v[100:103], v[76:79], v[60:63], v[100:103]\n" \
  "v_mov_b64 v[30:31], 0\n" \
  "v_mfma_f32_16x16x32_bf16 v[104:107], v[92:95], v[60:63], v[104:107]\n" \
  "v_mov_b64 v[32:33], 0\n" \
  "v_mov_b64 v[34:35], 0\n" \
  "v_mov_b64 v[36:37], 0\n" \
  "v_mov_b64 v[38:39], 0\n" \
  "v_mov_b64 v[40:41], 0\n" \
  "v_mov_b64 v[42:43], 0\n" \
  "v_max3_f32 v48, v96, v97, v100\n" \
  "v_max3_f32 v49, v101, v102, v103\n" \
  "v_max3_f32 v48, v48, v104, v105\n" \
  "v_max3_f32 v49, v49, v106, v107\n" \
  "v_max_f32 v48, v48, v49\n" \
  "v_mov_b32 v49, v48\n" \
  "v_mov_b64 v[44:45], 0\n" \
  "v_mov_b64 v[46:47], 0\n" \
  "v_permlane16_swap_b32 v48, v49\n" \
  "v_max_f32 v48, v48, v49\n" \
  "v_mov_b32 v49, v48\n" \
  "s_nop 1\n" \
  "v_permlane32_swap_b32 v48, v49\n" \
  "v_max_f32 v48, v48, v49\n" \
  "v_mul_f32 v48, 0xbe0293ee, v48\n" \
  "v_pk_fma_f32 v[100:101], v[100:101], s[80:81], v[48:49] op_sel_hi:[1,1,0]\n" \
  "v_pk_fma_f32 v[102:103], v[102:103], s[80:81], v[48:49] op_sel_hi:[1,1,0]\n" \
  "v_pk_fma_f32 v[104:105], v[104:105], s[80:81], v[48:49] op_sel_hi:[1,1,0]\n" \
  "v_pk_fma_f32 v[106:107], v[106:107], s[80:81], v[48:49] op_sel_hi:[1,1,0]\n" \
  "v_pk_fma_f32 v[96:97], v[96:97], s[80:81], v[48:49] op_sel_hi:[1,1,0]\n" \
  "v_exp_f32 v100, v100\n" \
  "v_exp_f32 v101, v101\n" \
  "v_exp_f32 v102, v102\n" \
  "v_exp_f32 v103, v103\n" \
  "v_exp_f32 v104, v104\n" \
  "v_exp_f32 v105, v105\n" \
  "v_exp_f32 v106, v106\n" \
  "v_exp_f32 v107, v107\n" \
  "v_exp_f32 v96, v96\n" \
  "v_exp_f32 v97, v97\n" \
  "v_cvt_pk_bf16_f32 v116, v100, v101\n" \
  "v_cvt_pk_bf16_f32 v117, v102, v103\n" \
  "v_cvt_pk_bf16_f32 v118, v104, v105\n" \
  "v_cvt_pk_bf16_f32 v119, v106, v107\n" \
  "v_cvt_pk_bf16_f32 v96, v96, v97\n" \
  "v_mov_b32 v97, 0\n" \
  "v_mfma_f32_16x16x32_bf16 v[52:55], v[124:127], v[116:119], 0\n" \
  "s_waitcnt lgkmcnt(1)\n" \
  "v_mfma_f32_16x16x32_bf16 v[100:103], v[108:111], v[116:119], 0\n" \
  "s_waitcnt vmcnt(1)\n" \
  "v_cmp_ne_u32 vcc, s70, v121\n" \
  "s_waitcnt lgkmcnt(0)\n" \
  "v_mfma_f32_16x16x32_bf16 v[104:107], v[112:115], v[116:119], 0\n" \
  "s_cbranch_vccnz HVM_MISS_%=\n" \
  "HVM_OK_%=:\n" \
  "v_mfma_f32_16x16x16_bf16 v[52:55], v[124:125], v[96:97], v[52:55]\n" \
  "v_mfma_f32_16x16x16_bf16 v[100:103], v[120:121], v[96:97], v[100:103]\n" \
  "v_mfma_f32_16x16x16_bf16 v[104:107], v[122:123], v[96:97], v[104:107]\n" \
  "s_add_u32 s59, s51, s54\n" \
  "s_add_u32 s57, s57, 262144\n" \
  "s_add_u32 s58, s58, 2048\n" \
  "s_add_u32 s48, s48, 1\n" \
  "s_add_u32 s49, s49, 16384\n" \
  "s_add_u32 s50, s50, 16384\n" \
  "v_rcp_f32 v52, v52\n" \
  "s_nop 0 ; auto\n" \
  "v_pk_mul_f32 v[100:101], v[100:101], v[52:53] op_sel_hi:[1,0]\n" \
  "v_pk_mul_f32 v[102:103], v[102:103], v[52:53] op_sel_hi:[1,0]\n" \
  "v_pk_mul_f32 v[104:105], v[104:105], v[52:53] op_sel_hi:[1,0]\n" \
  "v_pk_mul_f32 v[106:107], v[106:107], v[52:53] op_sel_hi:[1,0]\n" \
  "v_cvt_pk_bf16_f32 v56, v100, v104\n" \
  "v_cvt_pk_bf16_f32 v57, v101, v105\n" \
  "v_cvt_pk_bf16_f32 v58, v102, v106\n" \
  "v_cvt_pk_bf16_f32 v59, v103, v107\n" \
  "ds_write_b128 v5, v[56:59]\n" \
  "s_waitcnt lgkmcnt(0)\n" \
  "s_barrier\n" \
  "s_waitcnt vmcnt(0)\n" \
  "ds_read_b128 v[64:67], v0 offset:36864\n" \
  "ds_read_b128 v[68:71], v0 offset:37888\n" \
  "ds_read_b128 v[108:111], v0 offset:39936\n" \
  "s_waitcnt lgkmcnt(2)\n" \
  "v_smfmac_f32_16x16x64_bf16 v[16:19], v[64:67], v[128:135], v1\n" \
  "v_smfmac_f32_16x16x64_bf16 v[20:23], v[64:67], v[144:151], v1\n" \
  "s_waitcnt lgkmcnt(1)\n" \
  "v_smfmac_f32_16x16x64_bf16 v[16:19], v[68:71], v[136:143], v1\n" \
  "v_smfmac_f32_16x16x64_bf16 v[20:23], v[68:71], v[152:159], v1\n" \
  "v_smfmac_f32_16x16x64_bf16 v[24:27], v[64:67], v[160:167], v1\n" \
  "v_smfmac_f32_16x16x64_bf16 v[28:31], v[64:67], v[176:183], v1\n" \
  "v_smfmac_f32_16x16x64_bf16 v[24:27], v[68:71], v[168:175], v1\n" \
  "v_smfmac_f32_16x16x64_bf16 v[28:31], v[68:71], v[184:191], v1\n" \
  "v_smfmac_f32_16x16x64_bf16 v[32:35], v[64:67], v[192:199], v1\n" \
  "s_nop 1 ; auto\n" \
  "v_pk_add_f32 v[80:81], v[16:17], v[18:19]\n" \
  "v_smfmac_f32_16x16x64_bf16 v[36:39], v[64:67], v[208:215], v1\n" \
  "v_cvt_pk_bf16_f32 v72, v80, v81\n" \
  "v_smfmac_f32_16x16x64_bf16 v[32:35], v[68:71], v[200:207], v1\n" \
  "v_pk_add_f32 v[82:83], v[20:21], v[22:23]\n" \
  "v_smfmac_f32_16x16x64_bf16 v[36:39], v[68:71], v[216:223], v1\n" \
  "v_cvt_pk_bf16_f32 v73, v82, v83\n" \
  "v_smfmac_f32_16x16x64_bf16 v[40:43], v[64:67], v[224:231], v1\n" \
  "v_pk_add_f32 v[84:85], v[24:25], v[26:27]\n" \
  "v_smfmac_f32_16x16x64_bf16 v[44:47], v[64:67], v[240:247], v1\n" \
  "v_cvt_pk_bf16_f32 v74, v84, v85\n" \
  "v_smfmac_f32_16x16x64_bf16 v[40:43], v[68:71], v[232:239], v1\n" \
  "v_pk_add_f32 v[86:87], v[28:29], v[30:31]\n" \
  "v_smfmac_f32_16x16x64_bf16 v[44:47], v[68:71], v[248:255], v1\n" \
  "v_cvt_pk_bf16_f32 v75, v86, v87\n" \
  "v_bfi_b32 v72, 1, s82, v72\n" \
  "buffer_store_dwordx4 v[72:75], v7, s[40:43], s59 offen sc1\n" \
  "v_pk_add_f32 v[88:89], v[32:33], v[34:35]\n" \
  "v_cvt_pk_bf16_f32 v76, v88, v89\n" \
  "v_pk_add_f32 v[90:91], v[36:37], v[38:39]\n" \
  "v_cvt_pk_bf16_f32 v77, v90, v91\n" \
  "v_pk_add_f32 v[92:93], v[40:41], v[42:43]\n" \
  "v_cvt_pk_bf16_f32 v78, v92, v93\n" \
  "v_pk_add_f32 v[94:95], v[44:45], v[46:47]\n" \
  "v_cvt_pk_bf16_f32 v79, v94, v95\n" \
  "v_bfi_b32 v76, 1, s82, v76\n" \
  "buffer_store_dwordx4 v[76:79], v7, s[40:43], s59 offen offset:1024 sc1\n" \
  ".rept %c[wa]\n" \
  "s_nop 7\n" \
  ".endr\n" \
  "buffer_load_dwordx4 v[48:51], v8, s[40:43], s51 offen sc1\n" \
  "s_add_u32 s60, s51, 262144\n" \
  "buffer_load_dwordx4 v[52:55], v8, s[40:43], s60 offen sc1\n" \
  ".rept %c[wd]\n" \
  "s_nop 7\n" \
  ".endr\n" \
  "buffer_load_dwordx4 v[56:59], v8, s[40:43], s51 offen sc1\n" \
  "s_add_u32 s60, s51, 262144\n" \
  "buffer_load_dwordx4 v[60:63], v8, s[40:43], s60 offen sc1\n" \
  ".rept %c[wd]\n" \
  "s_nop 7\n" \
  ".endr\n" \
  "buffer_load_dwordx4 v[64:67], v8, s[40:43], s51 offen sc1\n" \
  "s_add_u32 s60, s51, 262144\n" \
  "buffer_load_dwordx4 v[68:71], v8, s[40:43], s60 offen sc1\n" \
  "s_waitcnt vmcnt(4)\n" \
  "v_xor_b32 v100, s82, v48\n" \
  "v_xor_b32 v101, s82, v52\n" \
  "s_waitcnt lgkmcnt(0)\n" \
  "v_mfma_f32_16x16x32_bf16 v[96:99], v[108:111], v[48:51], 0\n" \
  "v_or_b32 v100, v100, v101\n" \
  "v_and_b32 v100, 1, v100\n" \
  "v_mfma_f32_16x16x32_bf16 v[96:99], v[108:111], v[52:55], v[96:99]\n" \
  "v_cmp_ne_u32 vcc, 0, v100\n" \
  "s_cbranch_vccnz HRETRY_A_%=\n" \
  "HRED_OK_%=:\n" \
  "s_cmp_lg_u32 s66, 0\n" \
  "s_cbranch_scc1 HNOWIT_%=\n" \
  "v_mov_b32 v100, s48\n" \
  "s_mov_b64 exec, 1\n" \
  "buffer_store_dword v100, off, s[40:43], s78 sc1\n" \
  "s_mov_b64 exec, -1\n" \
  "HNOWIT_%=:\n" \
  "s_nop 4\n" \
  "v_add_f32_dpp v96, v96, v96 row_ror:4 row_mask:0xf bank_mask:0xf\n" \
  "v_add_f32_dpp v97, v97, v97 row_ror:4 row_mask:0xf bank_mask:0xf\n" \
  "s_nop 0 ; auto\n" \
  "v_add_f32_dpp v96, v96, v96 row_ror:8 row_mask:0xf bank_mask:0xf\n" \
  "v_add_f32_dpp v97, v97, v97 row_ror:8 row_mask:0xf bank_mask:0xf\n" \
  "v_cvt_pk_bf16_f32 v96, v96, v97\n" \
  "v_min_u32 v96, -2, v96\n" \
  "s_nop 1 ; auto\n" \
  "v_mov_b32_dpp v97, v96 row_shl:1 row_mask:0xf bank_mask:0xf\n" \
  "v_mov_b32_dpp v98, v96 row_shl:2 row_mask:0xf bank_mask:0xf\n" \
  "v_mov_b32_dpp v99, v96 row_shl:3 row_mask:0xf bank_mask:0xf\n" \
  "buffer_store_dwordx4 v[96:99], v9, s[40:43], s50 offen sc1\n" \
  "s_cmp_eq_u32 s48, 63\n" \
  "s_cbranch_scc1 HEND_%=\n" \
  ".rept %c[wb]\n" \
  "s_nop 7\n" \
  ".endr\n" \
  "s_branch HLOOP_%=\n" \
  "HRETRY_A_%=:\n" \
  "HRA_LOOP_%=:\n" \
  "s_nop 7\n" \
  "buffer_load_dwordx4 v[48:51], v8, s[40:43], s51 offen sc1\n" \
  "s_add_u32 s60, s51, 262144\n" \
  "buffer_load_dwordx4 v[52:55], v8, s[40:43], s60 offen sc1\n" \
  "s_waitcnt vmcnt(4)\n" \
  "v_xor_b32 v100, s82, v56\n" \
  "v_xor_b32 v101, s82, v60\n" \
  "v_mfma_f32_16x16x32_bf16 v[96:99], v[108:111], v[56:59], 0\n" \
  "v_or_b32 v100, v100, v101\n" \
  "v_and_b32 v100, 1, v100\n" \
  "v_mfma_f32_16x16x32_bf16 v[96:99], v[108:111], v[60:63], v[96:99]\n" \
  "v_cmp_ne_u32 vcc, 0, v100\n" \
  "s_cbranch_vccz HRED_OK_%=\n" \
  "s_nop 7\n" \
  "buffer_load_dwordx4 v[56:59], v8, s[40:43], s51 offen sc1\n" \
  "s_add_u32 s60, s51, 262144\n" \
  "buffer_load_dwordx4 v[60:63], v8, s[40:43], s60 offen sc1\n" \
  "s_waitcnt vmcnt(4)\n" \
  "v_xor_b32 v100, s82, v64\n" \
  "v_xor_b32 v101, s82, v68\n" \
  "v_mfma_f32_16x16x32_bf16 v[96:99], v[108:111], v[64:67], 0\n" \
  "v_or_b32 v100, v100, v101\n" \
  "v_and_b32 v100, 1, v100\n" \
  "v_mfma_f32_16x16x32_bf16 v[96:99], v[108:111], v[68:71], v[96:99]\n" \
  "v_cmp_ne_u32 vcc, 0, v100\n" \
  "s_cbranch_vccz HRED_OK_%=\n" \
  "s_nop 7\n" \
  "buffer_load_dwordx4 v[64:67], v8, s[40:43], s51 offen sc1\n" \
  "s_add_u32 s60, s51, 262144\n" \
  "buffer_load_dwordx4 v[68:71], v8, s[40:43], s60 offen sc1\n" \
  "s_waitcnt vmcnt(4)\n" \
  "v_xor_b32 v100, s82, v48\n" \
  "v_xor_b32 v101, s82, v52\n" \
  "v_mfma_f32_16x16x32_bf16 v[96:99], v[108:111], v[48:51], 0\n" \
  "v_or_b32 v100, v100, v101\n" \
  "v_and_b32 v100, 1, v100\n" \
  "v_mfma_f32_16x16x32_bf16 v[96:99], v[108:111], v[52:55], v[96:99]\n" \
  "v_cmp_ne_u32 vcc, 0, v100\n" \
  "s_cbranch_vccz HRED_OK_%=\n" \
  "s_add_u32 s61, s61, 1\n" \
  "s_cmp_gt_u32 s61, 0x40000\n" \
  "s_cbranch_scc1 HABORT_%=\n" \
  "s_branch HRA_LOOP_%=\n" \
  "HVM_MISS_%=:\n" \
  "s_waitcnt vmcnt(0)\n" \
  "v_cmp_ne_u32 vcc, s70, v65\n" \
  "v_mov_b64 v[120:121], v[64:65]\n" \
  "v_mov_b64 v[122:123], v[66:67]\n" \
  "s_nop 1\n" \
  "s_cbranch_vccz HVM_OK_%=\n" \
  "s_mov_b32 s61, 0\n" \
  "HVM_LOOP_%=:\n" \
  "buffer_load_dwordx4 v[120:123], v0, s[40:43], s72 offen sc1\n" \
  "s_waitcnt vmcnt(0)\n" \
  "v_cmp_ne_u32 vcc, s70, v121\n" \
  "s_cbranch_vccz HVM_OK_%=\n" \
  "s_add_u32 s61, s61, 1\n" \
  "s_cmp_gt_u32 s61, 0x40000\n" \
  "s_cbranch_scc1 HABORT_%=\n" \
  "s_branch HVM_LOOP_%=\n" \
  "HKVWAIT_%=:\n" \
  "s_add_u32 s70, s58, 2048\n" \
  "HKVWAIT1_%=:\n" \
  "buffer_load_dword v10, off, s[40:43], s70 sc1\n" \
  "s_waitcnt vmcnt(0)\n" \
  "v_readfirstlane_b32 s60, v10\n" \
  "s_nop 3 ; auto\n" \
  "s_cmp_eq_u32 s60, 4\n" \
  "s_cbranch_scc1 HKV_OK_%=\n" \
  "s_add_u32 s61, s61, 1\n" \
  "s_cmp_gt_u32 s61, 0x40000\n" \
  "s_cbranch_scc1 HABORT_%=\n" \
  "s_sleep 4\n" \
  "s_branch HKVWAIT1_%=\n" \
  "HMB_MISS_%=:\n" \
  "HMB_LOOP_%=:\n" \
  "buffer_load_dwordx4 v[112:115], v0, s[40:43], s49 offen sc1\n" \
  "buffer_load_dwordx4 v[116:119], v0, s[40:43], s49 offen offset:1024 sc1\n" \
  "s_waitcnt vmcnt(4)\n" \
  "v_max3_u32 v47, v96, v97, v98\n" \
  "v_max3_u32 v47, v47, v99, v100\n" \
  "v_max3_u32 v47, v47, v101, v102\n" \
  "v_max_u32 v47, v47, v103\n" \
  "v_cmp_eq_u32 vcc, -1, v47\n" \
  "s_cbranch_vccz HMB_GOT1_%=\n" \
  "buffer_load_dwordx4 v[96:99], v0, s[40:43], s49 offen sc1\n" \
  "buffer_load_dwordx4 v[100:103], v0, s[40:43], s49 offen offset:1024 sc1\n" \
  "s_waitcnt vmcnt(4)\n" \
  "v_max3_u32 v47, v104, v105, v106\n" \
  "v_max3_u32 v47, v47, v107, v108\n" \
  "v_max3_u32 v47, v47, v109, v110\n" \
  "v_max_u32 v47, v47, v111\n" \
  "v_cmp_eq_u32 vcc, -1, v47\n" \
  "s_cbranch_vccz HMB_GOT2_%=\n" \
  "buffer_load_dwordx4 v[104:107], v0, s[40:43], s49 offen sc1\n" \
  "buffer_load_dwordx4 v[108:111], v0, s[40:43], s49 offen offset:1024 sc1\n" \
  "s_waitcnt vmcnt(4)\n" \
  "v_max3_u32 v47, v112, v113, v114\n" \
  "v_max3_u32 v47, v47, v115, v116\n" \
  "v_max3_u32 v47, v47, v117, v118\n" \
  "v_max_u32 v47, v47, v119\n" \
  "v_cmp_eq_u32 vcc, -1, v47\n" \
  "s_cbranch_vccz HMB_GOT3_%=\n" \
  "s_add_u32 s61, s61, 1\n" \
  "s_cmp_gt_u32 s61, 0x40000\n" \
  "s_cbranch_scc1 HABORT_%=\n" \
  "s_branch HMB_LOOP_%=\n" \
  "HMB_GOT1_%=:\n" \
  "v_mov_b64 v[16:17], v[96:97]\n" \
  "v_mov_b64 v[18:19], v[98:99]\n" \
  "v_mov_b64 v[20:21], v[100:101]\n" \
  "v_mov_b64 v[22:23], v[102:103]\n" \
  "s_nop 1\n" \
  "s_branch HMB_OK_%=\n" \
  "HMB_GOT2_%=:\n" \
  "v_mov_b64 v[16:17], v[104:105]\n" \
  "v_mov_b64 v[18:19], v[106:107]\n" \
  "v_mov_b64 v[20:21], v[108:109]\n" \
  "v_mov_b64 v[22:23], v[110:111]\n" \
  "s_nop 1\n" \
  "s_branch HMB_OK_%=\n" \
  "HMB_GOT3_%=:\n" \
  "v_mov_b64 v[16:17], v[112:113]\n" \
  "v_mov_b64 v[18:19], v[114:115]\n" \
  "v_mov_b64 v[20:21], v[116:117]\n" \
  "v_mov_b64 v[22:23], v[118:119]\n" \
  "s_nop 1\n" \
  "s_branch HMB_OK_%=\n" \
  "HABORT_%=:\n" \
  "s_mov_b32 s59, 0x400000\n" \
  "s_mov_b64 exec, 1\n" \
  "buffer_store_dword v11, off, s[40:43], s59 sc1\n" \
  "s_mov_b64 exec, -1\n" \
  "HEND_%=:\n" \
  "s_mov_b64 exec, -1\n" \
  "s_waitcnt vmcnt(0) lgkmcnt(0)\n" \
  ""
#define HEAD_ASM_CLOB "v0", "v1", "v2", "v3", "v4", "v5", "v6", "v7", "v8", "v9", "v10", "v11", "v12", "v13", "v14", "v15", "v16", "v17", "v18", "v19", "v20", "v21", "v22", "v23", "v24", "v25", "v26", "v27", "v28", "v29", "v30", "v31", "v32", "v33", "v34", "v35", "v36", "v37", "v38", "v39", "v40", "v41", "v42", "v43", "v44", "v45", "v46", "v47", "v48", "v49", "v50", "v51", "v52", "v53", "v54", "v55", "v56", "v57", "v58", "v59", "v60", "v61", "v62", "v63", "v64", "v65", "v66", "v67", "v68", "v69", "v70", "v71", "v72", "v73", "v74", "v75", "v76", "v77", "v78", "v79", "v80", "v81", "v82", "v83", "v84", "v85", "v86", "v87", "v88", "v89", "v90", "v91", "v92", "v93", "v94", "v95", "v96", "v97", "v98", "v99", "v100", "v101", "v102", "v103", "v104", "v105", "v106", "v107", "v108", "v109", "v110", "v111", "v112", "v113", "v114", "v115", "v116", "v117", "v118", "v119", "v120", "v121", "v122", "v123", "v124", "v125", "v126", "v127", "v128", "v129", "v130", "v131", "v132", "v133", "v134", "v135", "v136", "v137", "v138", "v139", "v140", "v141", "v142", "v143", "v144", "v145", "v146", "v147", "v148", "v149", "v150", "v151", "v152", "v153", "v154", "v155", "v156", "v157", "v158", "v159", "v160", "v161", "v162", "v163", "v164", "v165", "v166", "v167", "v168", "v169", "v170", "v171", "v172", "v173", "v174", "v175", "v176", "v177", "v178", "v179", "v180", "v181", "v182", "v183", "v184", "v185", "v186", "v187", "v188", "v189", "v190", "v191", "v192", "v193", "v194", "v195", "v196", "v197", "v198", "v199", "v200", "v201", "v202", "v203", "v204", "v205", "v206", "v207", "v208", "v209", "v210", "v211", "v212", "v213", "v214", "v215", "v216", "v217", "v218", "v219", "v220", "v221", "v222", "v223", "v224", "v225", "v226", "v227", "v228", "v229", "v230", "v231", "v232", "v233", "v234", "v235", "v236", "v237", "v238", "v239", "v240", "v241", "v242", "v243", "v244", "v245", "v246", "v247", "v248", "v249", "v250", "v251", "v252", "v253", "v254", "v255", "a0", "a1", "a2", "a3", "a4", "a5", "a6", "a7", "a8", "a9", "a10", "a11", "a12", "a13", "a14", "a15", "a16", "a17", "a18", "a19", "a20", "a21", "a22", "a23", "a24", "a25", "a26", "a27", "a28", "a29", "a30", "a31", "a32", "a33", "a34", "a35", "a36", "a37", "a38", "a39", "a40", "a41", "a42", "a43", "a44", "a45", "a46", "a47", "a48", "a49", "a50", "a51", "a52", "a53", "a54", "a55", "a56", "a57", "a58", "a59", "a60", "a61", "a62", "a63", "a64", "a65", "a66", "a67", "a68", "a69", "a70", "a71", "a72", "a73", "a74", "a75", "a76", "a77", "a78", "a79", "a80", "a81", "a82", "a83", "a84", "a85", "a86", "a87", "a88", "a89", "a90", "a91", "a92", "a93", "a94", "a95", "a96", "a97", "a98", "a99", "a100", "a101", "a102", "a103", "a104", "a105", "a106", "a107", "a108", "a109", "a110", "a111", "a112", "a113", "a114", "a115", "a116", "a117", "a118", "a119", "a120", "a121", "a122", "a123", "a124", "a125", "a126", "a127", "a128", "a129", "a130", "a131", "a132", "a133", "a134", "a135", "a136", "a137", "a138", "a139", "a140", "a141", "a142", "a143", "a144", "a145", "a146", "a147", "a148", "a149", "a150", "a151", "a152", "a153", "a154", "a155", "a156", "a157", "a158", "a159", "a160", "a161", "a162", "a163", "a164", "a165", "a166", "a167", "a168", "a169", "a170", "a171", "a172", "a173", "a174", "a175", "a176", "a177", "a178", "a179", "a180", "a181", "a182", "a183", "a184", "a185", "a186", "a187", "a188", "a189", "a190", "a191", "a192", "a193", "a194", "a195", "a196", "a197", "a198", "a199", "a200", "a201", "a202", "a203", "a204", "a205", "a206", "a207", "a208", "a209", "a210", "a211", "a212", "a213", "a214", "a215", "a216", "a217", "a218", "a219", "a220", "a221", "a222", "a223", "a224", "a225", "a226", "a227", "a228", "a229", "a230", "a231", "a232", "a233", "a234", "a235", "a236", "a237", "a238", "a239", "a240", "a241", "a242", "a243", "a244", "a245", "a246", "a247", "a248", "a249", "a250", "a251", "a252", "a253", "a254", "a255", "s40", "s41", "s42", "s43", "s44", "s45", "s46", "s47", "s48", "s49", "s50", "s51", "s52", "s53", "s54", "s55", "s56", "s57", "s58", "s59", "s60", "s61", "s62", "s63", "s64", "s65", "s66", "s67", "s68", "s69", "s70", "s71", "s72", "s73", "s74", "s75", "s76", "s77", "s78", "s79", "s80", "s81", "s82", "s83", "s84", "s85", "vcc", "scc", "memory"

#define VHEAD_ASM \
  "s_mov_b32 s40, %[wslo]\n" \
  "s_mov_b32 s41, %[wshi]\n" \
  "s_mov_b32 s42, %[wssz]\n" \
  "s_mov_b32 s43, 0x00020000\n" \
  "s_mov_b32 s49, %[mem0]\n" \
  "s_mov_b32 s50, %[mem0]\n" \
  "s_mov_b32 s64, %[h]\n" \
  "s_mov_b32 s66, %[w]\n" \
  "s_mov_b32 s78, %[wit]\n" \
  "s_mov_b32 s79, %[vf0]\n" \
  "v_mbcnt_lo_u32_b32 v16, -1, 0\n" \
  "v_mbcnt_hi_u32_b32 v16, -1, v16\n" \
  "v_lshlrev_b32 v16, 2, v16\n" \
  "s_mul_i32 s59, s66, 4096\n" \
  "s_add_u32 s59, s59, 49152\n" \
  "v_add_u32 v16, s59, v16\n" \
  "ds_read_b32 v0, v16 offset:0\n" \
  "ds_read_b32 v1, v16 offset:256\n" \
  "ds_read_b32 v2, v16 offset:512\n" \
  "ds_read_b32 v3, v16 offset:768\n" \
  "ds_read_b32 v4, v16 offset:1024\n" \
  "ds_read_b32 v5, v16 offset:1280\n" \
  "ds_read_b32 v6, v16 offset:1536\n" \
  "ds_read_b32 v7, v16 offset:1792\n" \
  "ds_read_b32 v8, v16 offset:2048\n" \
  "ds_read_b32 v9, v16 offset:2304\n" \
  "ds_read_b32 v17, v16 offset:2560\n" \
  "ds_read_b32 v18, v16 offset:2816\n" \
  "ds_read_b32 v19, v16 offset:3072\n" \
  "s_waitcnt lgkmcnt(0)\n" \
  "s_lshl_b32 s54, s64, 14\n" \
  "s_lshl_b32 s59, s66, 11\n" \
  "s_add_u32 s54, s54, s59\n" \
  "s_lshl_b32 s55, s64, 12\n" \
  "s_lshl_b32 s59, s66, 10\n" \
  "s_add_u32 s55, s55, s59\n" \
  "s_add_u32 s55, s55, 0xa00000\n" \
  "s_add_u32 s79, s79, s59\n" \
  "s_add_u32 s73, s79, 262144\n" \
  "s_lshl_b32 s56, s64, 11\n" \
  "s_lshl_b32 s59, s66, 9\n" \
  "s_add_u32 s56, s56, s59\n" \
  "s_add_u32 s56, s56, 0x1200000\n" \
  "s_lshl_b32 s57, s64, 13\n" \
  "s_lshl_b32 s59, s66, 11\n" \
  "s_add_u32 s57, s57, s59\n" \
  "s_add_u32 s62, s59, 0\n" \
  "s_add_u32 s63, s59, 16384\n" \
  "s_add_u32 s49, s49, s59\n" \
  "s_add_u32 s84, s59, 40960\n" \
  "s_lshl_b32 s58, s64, 6\n" \
  "s_add_u32 s58, s58, 0x401000\n" \
  "s_lshl_b32 s67, s66, 2\n" \
  "s_mov_b32 s48, 0\n" \
  "s_mov_b32 s65, 0\n" \
  "s_lshr_b32 s85, s66, 1\n" \
  "s_lshl_b32 s85, s85, 7\n" \
  "s_and_b32 s83, s66, 1\n" \
  "s_lshl_b32 s83, s83, 11\n" \
  "v_mov_b32 v11, -1\n" \
  "s_add_u32 s59, s66, 0\n" \
  "s_and_b32 s59, s59, 3\n" \
  "s_lshl_b32 s74, s59, 20\n" \
  "s_add_u32 s59, s66, 1\n" \
  "s_and_b32 s59, s59, 3\n" \
  "s_lshl_b32 s75, s59, 20\n" \
  "s_lshl_b32 s59, s59, 11\n" \
  "s_add_u32 s59, s59, 40960\n" \
  "v_add_u32 v13, s59, v0\n" \
  "s_add_u32 s59, s66, 2\n" \
  "s_and_b32 s59, s59, 3\n" \
  "s_lshl_b32 s76, s59, 20\n" \
  "s_lshl_b32 s59, s59, 11\n" \
  "s_add_u32 s59, s59, 40960\n" \
  "v_add_u32 v14, s59, v0\n" \
  "s_add_u32 s59, s66, 3\n" \
  "s_and_b32 s59, s59, 3\n" \
  "s_lshl_b32 s77, s59, 20\n" \
  "s_lshl_b32 s59, s59, 11\n" \
  "s_add_u32 s59, s59, 40960\n" \
  "v_add_u32 v15, s59, v0\n" \
  "s_mov_b32 s80, 0x3e0293ee\n" \
  "s_mov_b32 s81, 0x3e0293ee\n" \
  "s_mov_b32 s68, 0x05040100\n" \
  "s_mov_b32 s69, 0x07060302\n" \
  "s_mov_b32 s44, %[wvlo]\n" \
  "s_mov_b32 s45, %[wvhi]\n" \
  "s_mov_b32 s46, 0x400000\n" \
  "s_mov_b32 s47, 0x00020000\n" \
  "s_add_u32 s59, s74, 0x0\n" \
  "buffer_load_dword v32, v19, s[44:47], s59 offen\n" \
  "s_add_u32 s59, s74, 0x2000\n" \
  "buffer_load_dword v33, v19, s[44:47], s59 offen\n" \
  "s_add_u32 s59, s74, 0x4000\n" \
  "buffer_load_dword v34, v19, s[44:47], s59 offen\n" \
  "s_add_u32 s59, s74, 0x6000\n" \
  "buffer_load_dword v35, v19, s[44:47], s59 offen\n" \
  "s_add_u32 s59, s74, 0x10000\n" \
  "buffer_load_dword v36, v19, s[44:47], s59 offen\n" \
  "s_add_u32 s59, s74, 0x12000\n" \
  "buffer_load_dword v37, v19, s[44:47], s59 offen\n" \
  "s_add_u32 s59, s74, 0x14000\n" \
  "buffer_load_dword v38, v19, s[44:47], s59 offen\n" \
  "s_add_u32 s59, s74, 0x16000\n" \
  "buffer_load_dword v39, v19, s[44:47], s59 offen\n" \
  "s_add_u32 s59, s74, 0x80000\n" \
  "buffer_load_dword v40, v19, s[44:47], s59 offen\n" \
  "s_add_u32 s59, s74, 0x82000\n" \
  "buffer_load_dword v41, v19, s[44:47], s59 offen\n" \
  "s_add_u32 s59, s74, 0x84000\n" \
  "buffer_load_dword v42, v19, s[44:47], s59 offen\n" \
  "s_add_u32 s59, s74, 0x86000\n" \
  "buffer_load_dword v43, v19, s[44:47], s59 offen\n" \
  "s_add_u32 s59, s74, 0x90000\n" \
  "buffer_load_dword v44, v19, s[44:47], s59 offen\n" \
  "s_add_u32 s59, s74, 0x92000\n" \
  "buffer_load_dword v45, v19, s[44:47], s59 offen\n" \
  "s_add_u32 s59, s74, 0x94000\n" \
  "buffer_load_dword v46, v19, s[44:47], s59 offen\n" \
  "s_add_u32 s59, s74, 0x96000\n" \
  "buffer_load_dword v47, v19, s[44:47], s59 offen\n" \
  "s_add_u32 s59, s74, 0x8000\n" \
  "buffer_load_dword v48, v19, s[44:47], s59 offen\n" \
  "s_add_u32 s59, s74, 0xa000\n" \
  "buffer_load_dword v49, v19, s[44:47], s59 offen\n" \
  "s_add_u32 s59, s74, 0xc000\n" \
  "buffer_load_dword v50, v19, s[44:47], s59 offen\n" \
  "s_add_u32 s59, s74, 0xe000\n" \
  "buffer_load_dword v51, v19, s[44:47], s59 offen\n" \
  "s_add_u32 s59, s74, 0x18000\n" \
  "buffer_load_dword v52, v19, s[44:47], s59 offen\n" \
  "s_add_u32 s59, s74, 0x1a000\n" \
  "buffer_load_dword v53, v19, s[44:47], s59 offen\n" \
  "s_add_u32 s59, s74, 0x1c000\n" \
  "buffer_load_dword v54, v19, s[44:47], s59 offen\n" \
  "s_add_u32 s59, s74, 0x1e000\n" \
  "buffer_load_dword v55, v19, s[44:47], s59 offen\n" \
  "s_add_u32 s59, s74, 0x88000\n" \
  "buffer_load_dword v56, v19, s[44:47], s59 offen\n" \
  "s_add_u32 s59, s74, 0x8a000\n" \
  "buffer_load_dword v57, v19, s[44:47], s59 offen\n" \
  "s_add_u32 s59, s74, 0x8c000\n" \
  "buffer_load_dword v58, v19, s[44:47], s59 offen\n" \
  "s_add_u32 s59, s74, 0x8e000\n" \
  "buffer_load_dword v59, v19, s[44:47], s59 offen\n" \
  "s_add_u32 s59, s74, 0x98000\n" \
  "buffer_load_dword v60, v19, s[44:47], s59 offen\n" \
  "s_add_u32 s59, s74, 0x9a000\n" \
  "buffer_load_dword v61, v19, s[44:47], s59 offen\n" \
  "s_add_u32 s59, s74, 0x9c000\n" \
  "buffer_load_dword v62, v19, s[44:47], s59 offen\n" \
  "s_add_u32 s59, s74, 0x9e000\n" \
  "buffer_load_dword v63, v19, s[44:47], s59 offen\n" \
  "s_add_u32 s59, s75, 0x0\n" \
  "buffer_load_dword v64, v19, s[44:47], s59 offen\n" \
  "s_add_u32 s59, s75, 0x2000\n" \
  "buffer_load_dword v65, v19, s[44:47], s59 offen\n" \
  "s_add_u32 s59, s75, 0x4000\n" \
  "buffer_load_dword v66, v19, s[44:47], s59 offen\n" \
  "s_add_u32 s59, s75, 0x6000\n" \
  "buffer_load_dword v67, v19, s[44:47], s59 offen\n" \
  "s_add_u32 s59, s75, 0x10000\n" \
  "buffer_load_dword v68, v19, s[44:47], s59 offen\n" \
  "s_add_u32 s59, s75, 0x12000\n" \
  "buffer_load_dword v69, v19, s[44:47], s59 offen\n" \
  "s_add_u32 s59, s75, 0x14000\n" \
  "buffer_load_dword v70, v19, s[44:47], s59 offen\n" \
  "s_add_u32 s59, s75, 0x16000\n" \
  "buffer_load_dword v71, v19, s[44:47], s59 offen\n" \
  "s_add_u32 s59, s75, 0x80000\n" \
  "buffer_load_dword v72, v19, s[44:47], s59 offen\n" \
  "s_add_u32 s59, s75, 0x82000\n" \
  "buffer_load_dword v73, v19, s[44:47], s59 offen\n" \
  "s_add_u32 s59, s75, 0x84000\n" \
  "buffer_load_dword v74, v19, s[44:47], s59 offen\n" \
  "s_add_u32 s59, s75, 0x86000\n" \
  "buffer_load_dword v75, v19, s[44:47], s59 offen\n" \
  "s_add_u32 s59, s75, 0x90000\n" \
  "buffer_load_dword v76, v19, s[44:47], s59 offen\n" \
  "s_add_u32 s59, s75, 0x92000\n" \
  "buffer_load_dword v77, v19, s[44:47], s59 offen\n" \
  "s_add_u32 s59, s75, 0x94000\n" \
  "buffer_load_dword v78, v19, s[44:47], s59 offen\n" \
  "s_add_u32 s59, s75, 0x96000\n" \
  "buffer_load_dword v79, v19, s[44:47], s59 offen\n" \
  "s_add_u32 s59, s75, 0x8000\n" \
  "buffer_load_dword v80, v19, s[44:47], s59 offen\n" \
  "s_add_u32 s59, s75, 0xa000\n" \
  "buffer_load_dword v81, v19, s[44:47], s59 offen\n" \
  "s_add_u32 s59, s75, 0xc000\n" \
  "buffer_load_dword v82, v19, s[44:47], s59 offen\n" \
  "s_add_u32 s59, s75, 0xe000\n" \
  "buffer_load_dword v83, v19, s[44:47], s59 offen\n" \
  "s_add_u32 s59, s75, 0x18000\n" \
  "buffer_load_dword v84, v19, s[44:47], s59 offen\n" \
  "s_add_u32 s59, s75, 0x1a000\n" \
  "buffer_load_dword v85, v19, s[44:47], s59 offen\n" \
  "s_add_u32 s59, s75, 0x1c000\n" \
  "buffer_load_dword v86, v19, s[44:47], s59 offen\n" \
  "s_add_u32 s59, s75, 0x1e000\n" \
  "buffer_load_dword v87, v19, s[44:47], s59 offen\n" \
  "s_add_u32 s59, s75, 0x88000\n" \
  "buffer_load_dword v88, v19, s[44:47], s59 offen\n" \
  "s_add_u32 s59, s75, 0x8a000\n" \
  "buffer_load_dword v89, v19, s[44:47], s59 offen\n" \
  "s_add_u32 s59, s75, 0x8c000\n" \
  "buffer_load_dword v90, v19, s[44:47], s59 offen\n" \
  "s_add_u32 s59, s75, 0x8e000\n" \
  "buffer_load_dword v91, v19, s[44:47], s59 offen\n" \
  "s_add_u32 s59, s75, 0x98000\n" \
  "buffer_load_dword v92, v19, s[44:47], s59 offen\n" \
  "s_add_u32 s59, s75, 0x9a000\n" \
  "buffer_load_dword v93, v19, s[44:47], s59 offen\n" \
  "s_add_u32 s59, s75, 0x9c000\n" \
  "buffer_load_dword v94, v19, s[44:47], s59 offen\n" \
  "s_add_u32 s59, s75, 0x9e000\n" \
  "buffer_load_dword v95, v19, s[44:47], s59 offen\n" \
  "s_waitcnt vmcnt(62)\n" \
  "v_perm_b32 v128, v33, v32, s68\n" \
  "v_perm_b32 v192, v33, v32, s69\n" \
  "s_waitcnt vmcnt(60)\n" \
  "v_perm_b32 v129, v35, v34, s68\n" \
  "v_perm_b32 v193, v35, v34, s69\n" \
  "s_waitcnt vmcnt(58)\n" \
  "v_perm_b32 v130, v37, v36, s68\n" \
  "v_perm_b32 v194, v37, v36, s69\n" \
  "s_waitcnt vmcnt(56)\n" \
  "v_perm_b32 v131, v39, v38, s68\n" \
  "v_perm_b32 v195, v39, v38, s69\n" \
  "s_waitcnt vmcnt(54)\n" \
  "v_perm_b32 v132, v41, v40, s68\n" \
  "v_perm_b32 v196, v41, v40, s69\n" \
  "s_waitcnt vmcnt(52)\n" \
  "v_perm_b32 v133, v43, v42, s68\n" \
  "v_perm_b32 v197, v43, v42, s69\n" \
  "s_waitcnt vmcnt(50)\n" \
  "v_perm_b32 v134, v45, v44, s68\n" \
  "v_perm_b32 v198, v45, v44, s69\n" \
  "s_waitcnt vmcnt(48)\n" \
  "v_perm_b32 v135, v47, v46, s68\n" \
  "v_perm_b32 v199, v47, v46, s69\n" \
  "s_add_u32 s59, s76, 0x0\n" \
  "buffer_load_dword v96, v19, s[44:47], s59 offen\n" \
  "s_add_u32 s59, s76, 0x2000\n" \
  "buffer_load_dword v97, v19, s[44:47], s59 offen\n" \
  "s_add_u32 s59, s76, 0x4000\n" \
  "buffer_load_dword v98, v19, s[44:47], s59 offen\n" \
  "s_add_u32 s59, s76, 0x6000\n" \
  "buffer_load_dword v99, v19, s[44:47], s59 offen\n" \
  "s_add_u32 s59, s76, 0x10000\n" \
  "buffer_load_dword v100, v19, s[44:47], s59 offen\n" \
  "s_add_u32 s59, s76, 0x12000\n" \
  "buffer_load_dword v101, v19, s[44:47], s59 offen\n" \
  "s_add_u32 s59, s76, 0x14000\n" \
  "buffer_load_dword v102, v19, s[44:47], s59 offen\n" \
  "s_add_u32 s59, s76, 0x16000\n" \
  "buffer_load_dword v103, v19, s[44:47], s59 offen\n" \
  "s_add_u32 s59, s76, 0x80000\n" \
  "buffer_load_dword v104, v19, s[44:47], s59 offen\n" \
  "s_add_u32 s59, s76, 0x82000\n" \
  "buffer_load_dword v105, v19, s[44:47], s59 offen\n" \
  "s_add_u32 s59, s76, 0x84000\n" \
  "buffer_load_dword v106, v19, s[44:47], s59 offen\n" \
  "s_add_u32 s59, s76, 0x86000\n" \
  "buffer_load_dword v107, v19, s[44:47], s59 offen\n" \
  "s_add_u32 s59, s76, 0x90000\n" \
  "buffer_load_dword v108, v19, s[44:47], s59 offen\n" \
  "s_add_u32 s59, s76, 0x92000\n" \
  "buffer_load_dword v109, v19, s[44:47], s59 offen\n" \
  "s_add_u32 s59, s76, 0x94000\n" \
  "buffer_load_dword v110, v19, s[44:47], s59 offen\n" \
  "s_add_u32 s59, s76, 0x96000\n" \
  "buffer_load_dword v111, v19, s[44:47], s59 offen\n" \
  "s_waitcnt vmcnt(62)\n" \
  "v_perm_b32 v136, v49, v48, s68\n" \
  "v_perm_b32 v200, v49, v48, s69\n" \
  "s_waitcnt vmcnt(60)\n" \
  "v_perm_b32 v137, v51, v50, s68\n" \
  "v_perm_b32 v201, v51, v50, s69\n" \
  "s_waitcnt vmcnt(58)\n" \
  "v_perm_b32 v138, v53, v52, s68\n" \
  "v_perm_b32 v202, v53, v52, s69\n" \
  "s_waitcnt vmcnt(56)\n" \
  "v_perm_b32 v139, v55, v54, s68\n" \
  "v_perm_b32 v203, v55, v54, s69\n" \
  "s_waitcnt vmcnt(54)\n" \
  "v_perm_b32 v140, v57, v56, s68\n" \
  "v_perm_b32 v204, v57, v56, s69\n" \
  "s_waitcnt vmcnt(52)\n" \
  "v_perm_b32 v141, v59, v58, s68\n" \
  "v_perm_b32 v205, v59, v58, s69\n" \
  "s_waitcnt vmcnt(50)\n" \
  "v_perm_b32 v142, v61, v60, s68\n" \
  "v_perm_b32 v206, v61, v60, s69\n" \
  "s_waitcnt vmcnt(48)\n" \
  "v_perm_b32 v143, v63, v62, s68\n" \
  "v_perm_b32 v207, v63, v62, s69\n" \
  "s_add_u32 s59, s76, 0x8000\n" \
  "buffer_load_dword v112, v19, s[44:47], s59 offen\n" \
  "s_add_u32 s59, s76, 0xa000\n" \
  "buffer_load_dword v113, v19, s[44:47], s59 offen\n" \
  "s_add_u32 s59, s76, 0xc000\n" \
  "buffer_load_dword v114, v19, s[44:47], s59 offen\n" \
  "s_add_u32 s59, s76, 0xe000\n" \
  "buffer_load_dword v115, v19, s[44:47], s59 offen\n" \
  "s_add_u32 s59, s76, 0x18000\n" \
  "buffer_load_dword v116, v19, s[44:47], s59 offen\n" \
  "s_add_u32 s59, s76, 0x1a000\n" \
  "buffer_load_dword v117, v19, s[44:47], s59 offen\n" \
  "s_add_u32 s59, s76, 0x1c000\n" \
  "buffer_load_dword v118, v19, s[44:47], s59 offen\n" \
  "s_add_u32 s59, s76, 0x1e000\n" \
  "buffer_load_dword v119, v19, s[44:47], s59 offen\n" \
  "s_add_u32 s59, s76, 0x88000\n" \
  "buffer_load_dword v120, v19, s[44:47], s59 offen\n" \
  "s_add_u32 s59, s76, 0x8a000\n" \
  "buffer_load_dword v121, v19, s[44:47], s59 offen\n" \
  "s_add_u32 s59, s76, 0x8c000\n" \
  "buffer_load_dword v122, v19, s[44:47], s59 offen\n" \
  "s_add_u32 s59, s76, 0x8e000\n" \
  "buffer_load_dword v123, v19, s[44:47], s59 offen\n" \
  "s_add_u32 s59, s76, 0x98000\n" \
  "buffer_load_dword v124, v19, s[44:47], s59 offen\n" \
  "s_add_u32 s59, s76, 0x9a000\n" \
  "buffer_load_dword v125, v19, s[44:47], s59 offen\n" \
  "s_add_u32 s59, s76, 0x9c000\n" \
  "buffer_load_dword v126, v19, s[44:47], s59 offen\n" \
  "s_add_u32 s59, s76, 0x9e000\n" \
  "buffer_load_dword v127, v19, s[44:47], s59 offen\n" \
  "s_waitcnt vmcnt(62)\n" \
  "v_perm_b32 v144, v65, v64, s68\n" \
  "v_perm_b32 v208, v65, v64, s69\n" \
  "s_waitcnt vmcnt(60)\n" \
  "v_perm_b32 v145, v67, v66, s68\n" \
  "v_perm_b32 v209, v67, v66, s69\n" \
  "s_waitcnt vmcnt(58)\n" \
  "v_perm_b32 v146, v69, v68, s68\n" \
  "v_perm_b32 v210, v69, v68, s69\n" \
  "s_waitcnt vmcnt(56)\n" \
  "v_perm_b32 v147, v71, v70, s68\n" \
  "v_perm_b32 v211, v71, v70, s69\n" \
  "s_waitcnt vmcnt(54)\n" \
  "v_perm_b32 v148, v73, v72, s68\n" \
  "v_perm_b32 v212, v73, v72, s69\n" \
  "s_waitcnt vmcnt(52)\n" \
  "v_perm_b32 v149, v75, v74, s68\n" \
  "v_perm_b32 v213, v75, v74, s69\n" \
  "s_waitcnt vmcnt(50)\n" \
  "v_perm_b32 v150, v77, v76, s68\n" \
  "v_perm_b32 v214, v77, v76, s69\n" \
  "s_waitcnt vmcnt(48)\n" \
  "v_perm_b32 v151, v79, v78, s68\n" \
  "v_perm_b32 v215, v79, v78, s69\n" \
  "s_add_u32 s59, s77, 0x0\n" \
  "buffer_load_dword v32, v19, s[44:47], s59 offen\n" \
  "s_add_u32 s59, s77, 0x2000\n" \
  "buffer_load_dword v33, v19, s[44:47], s59 offen\n" \
  "s_add_u32 s59, s77, 0x4000\n" \
  "buffer_load_dword v34, v19, s[44:47], s59 offen\n" \
  "s_add_u32 s59, s77, 0x6000\n" \
  "buffer_load_dword v35, v19, s[44:47], s59 offen\n" \
  "s_add_u32 s59, s77, 0x10000\n" \
  "buffer_load_dword v36, v19, s[44:47], s59 offen\n" \
  "s_add_u32 s59, s77, 0x12000\n" \
  "buffer_load_dword v37, v19, s[44:47], s59 offen\n" \
  "s_add_u32 s59, s77, 0x14000\n" \
  "buffer_load_dword v38, v19, s[44:47], s59 offen\n" \
  "s_add_u32 s59, s77, 0x16000\n" \
  "buffer_load_dword v39, v19, s[44:47], s59 offen\n" \
  "s_add_u32 s59, s77, 0x80000\n" \
  "buffer_load_dword v40, v19, s[44:47], s59 offen\n" \
  "s_add_u32 s59, s77, 0x82000\n" \
  "buffer_load_dword v41, v19, s[44:47], s59 offen\n" \
  "s_add_u32 s59, s77, 0x84000\n" \
  "buffer_load_dword v42, v19, s[44:47], s59 offen\n" \
  "s_add_u32 s59, s77, 0x86000\n" \
  "buffer_load_dword v43, v19, s[44:47], s59 offen\n" \
  "s_add_u32 s59, s77, 0x90000\n" \
  "buffer_load_dword v44, v19, s[44:47], s59 offen\n" \
  "s_add_u32 s59, s77, 0x92000\n" \
  "buffer_load_dword v45, v19, s[44:47], s59 offen\n" \
  "s_add_u32 s59, s77, 0x94000\n" \
  "buffer_load_dword v46, v19, s[44:47], s59 offen\n" \
  "s_add_u32 s59, s77, 0x96000\n" \
  "buffer_load_dword v47, v19, s[44:47], s59 offen\n" \
  "s_waitcnt vmcnt(62)\n" \
  "v_perm_b32 v152, v81, v80, s68\n" \
  "v_perm_b32 v216, v81, v80, s69\n" \
  "s_waitcnt vmcnt(60)\n" \
  "v_perm_b32 v153, v83, v82, s68\n" \
  "v_perm_b32 v217, v83, v82, s69\n" \
  "s_waitcnt vmcnt(58)\n" \
  "v_perm_b32 v154, v85, v84, s68\n" \
  "v_perm_b32 v218, v85, v84, s69\n" \
  "s_waitcnt vmcnt(56)\n" \
  "v_perm_b32 v155, v87, v86, s68\n" \
  "v_perm_b32 v219, v87, v86, s69\n" \
  "s_waitcnt vmcnt(54)\n" \
  "v_perm_b32 v156, v89, v88, s68\n" \
  "v_perm_b32 v220, v89, v88, s69\n" \
  "s_waitcnt vmcnt(52)\n" \
  "v_perm_b32 v157, v91, v90, s68\n" \
  "v_perm_b32 v221, v91, v90, s69\n" \
  "s_waitcnt vmcnt(50)\n" \
  "v_perm_b32 v158, v93, v92, s68\n" \
  "v_perm_b32 v222, v93, v92, s69\n" \
  "s_waitcnt vmcnt(48)\n" \
  "v_perm_b32 v159, v95, v94, s68\n" \
  "v_perm_b32 v223, v95, v94, s69\n" \
  "s_add_u32 s59, s77, 0x8000\n" \
  "buffer_load_dword v48, v19, s[44:47], s59 offen\n" \
  "s_add_u32 s59, s77, 0xa000\n" \
  "buffer_load_dword v49, v19, s[44:47], s59 offen\n" \
  "s_add_u32 s59, s77, 0xc000\n" \
  "buffer_load_dword v50, v19, s[44:47], s59 offen\n" \
  "s_add_u32 s59, s77, 0xe000\n" \
  "buffer_load_dword v51, v19, s[44:47], s59 offen\n" \
  "s_add_u32 s59, s77, 0x18000\n" \
  "buffer_load_dword v52, v19, s[44:47], s59 offen\n" \
  "s_add_u32 s59, s77, 0x1a000\n" \
  "buffer_load_dword v53, v19, s[44:47], s59 offen\n" \
  "s_add_u32 s59, s77, 0x1c000\n" \
  "buffer_load_dword v54, v19, s[44:47], s59 offen\n" \
  "s_add_u32 s59, s77, 0x1e000\n" \
  "buffer_load_dword v55, v19, s[44:47], s59 offen\n" \
  "s_add_u32 s59, s77, 0x88000\n" \
  "buffer_load_dword v56, v19, s[44:47], s59 offen\n" \
  "s_add_u32 s59, s77, 0x8a000\n" \
  "buffer_load_dword v57, v19, s[44:47], s59 offen\n" \
  "s_add_u32 s59, s77, 0x8c000\n" \
  "buffer_load_dword v58, v19, s[44:47], s59 offen\n" \
  "s_add_u32 s59, s77, 0x8e000\n" \
  "buffer_load_dword v59, v19, s[44:47], s59 offen\n" \
  "s_add_u32 s59, s77, 0x98000\n" \
  "buffer_load_dword v60, v19, s[44:47], s59 offen\n" \
  "s_add_u32 s59, s77, 0x9a000\n" \
  "buffer_load_dword v61, v19, s[44:47], s59 offen\n" \
  "s_add_u32 s59, s77, 0x9c000\n" \
  "buffer_load_dword v62, v19, s[44:47], s59 offen\n" \
  "s_add_u32 s59, s77, 0x9e000\n" \
  "buffer_load_dword v63, v19, s[44:47], s59 offen\n" \
  "s_mov_b32 s44, %[wklo]\n" \
  "s_mov_b32 s45, %[wkhi]\n" \
  "s_mov_b32 s46, 0x400000\n" \
  "s_mov_b32 s47, 0x00020000\n" \
  "s_add_u32 s59, s74, 0x0\n" \
  "buffer_load_dword v64, v19, s[44:47], s59 offen\n" \
  "s_add_u32 s59, s74, 0x2000\n" \
  "buffer_load_dword v65, v19, s[44:47], s59 offen\n" \
  "s_add_u32 s59, s74, 0x4000\n" \
  "buffer_load_dword v66, v19, s[44:47], s59 offen\n" \
  "s_add_u32 s59, s74, 0x6000\n" \
  "buffer_load_dword v67, v19, s[44:47], s59 offen\n" \
  "s_add_u32 s59, s74, 0x10000\n" \
  "buffer_load_dword v68, v19, s[44:47], s59 offen\n" \
  "s_add_u32 s59, s74, 0x12000\n" \
  "buffer_load_dword v69, v19, s[44:47], s59 offen\n" \
  "s_add_u32 s59, s74, 0x14000\n" \
  "buffer_load_dword v70, v19, s[44:47], s59 offen\n" \
  "s_add_u32 s59, s74, 0x16000\n" \
  "buffer_load_dword v71, v19, s[44:47], s59 offen\n" \
  "s_add_u32 s59, s74, 0x80000\n" \
  "buffer_load_dword v72, v19, s[44:47], s59 offen\n" \
  "s_add_u32 s59, s74, 0x82000\n" \
  "buffer_load_dword v73, v19, s[44:47], s59 offen\n" \
  "s_add_u32 s59, s74, 0x84000\n" \
  "buffer_load_dword v74, v19, s[44:47], s59 offen\n" \
  "s_add_u32 s59, s74, 0x86000\n" \
  "buffer_load_dword v75, v19, s[44:47], s59 offen\n" \
  "s_add_u32 s59, s74, 0x90000\n" \
  "buffer_load_dword v76, v19, s[44:47], s59 offen\n" \
  "s_add_u32 s59, s74, 0x92000\n" \
  "buffer_load_dword v77, v19, s[44:47], s59 offen\n" \
  "s_add_u32 s59, s74, 0x94000\n" \
  "buffer_load_dword v78, v19, s[44:47], s59 offen\n" \
  "s_add_u32 s59, s74, 0x96000\n" \
  "buffer_load_dword v79, v19, s[44:47], s59 offen\n" \
  "s_waitcnt vmcnt(63)\n" \
  "v_perm_b32 v160, v97, v96, s68\n" \
  "v_perm_b32 v224, v97, v96, s69\n" \
  "v_perm_b32 v161, v99, v98, s68\n" \
  "v_perm_b32 v225, v99, v98, s69\n" \
  "v_perm_b32 v162, v101, v100, s68\n" \
  "v_perm_b32 v226, v101, v100, s69\n" \
  "v_perm_b32 v163, v103, v102, s68\n" \
  "v_perm_b32 v227, v103, v102, s69\n" \
  "v_perm_b32 v164, v105, v104, s68\n" \
  "v_perm_b32 v228, v105, v104, s69\n" \
  "v_perm_b32 v165, v107, v106, s68\n" \
  "v_perm_b32 v229, v107, v106, s69\n" \
  "v_perm_b32 v166, v109, v108, s68\n" \
  "v_perm_b32 v230, v109, v108, s69\n" \
  "v_perm_b32 v167, v111, v110, s68\n" \
  "v_perm_b32 v231, v111, v110, s69\n" \
  "s_add_u32 s59, s74, 0x8000\n" \
  "buffer_load_dword v80, v19, s[44:47], s59 offen\n" \
  "s_add_u32 s59, s74, 0xa000\n" \
  "buffer_load_dword v81, v19, s[44:47], s59 offen\n" \
  "s_add_u32 s59, s74, 0xc000\n" \
  "buffer_load_dword v82, v19, s[44:47], s59 offen\n" \
  "s_add_u32 s59, s74, 0xe000\n" \
  "buffer_load_dword v83, v19, s[44:47], s59 offen\n" \
  "s_add_u32 s59, s74, 0x18000\n" \
  "buffer_load_dword v84, v19, s[44:47], s59 offen\n" \
  "s_add_u32 s59, s74, 0x1a000\n" \
  "buffer_load_dword v85, v19, s[44:47], s59 offen\n" \
  "s_add_u32 s59, s74, 0x1c000\n" \
  "buffer_load_dword v86, v19, s[44:47], s59 offen\n" \
  "s_add_u32 s59, s74, 0x1e000\n" \
  "buffer_load_dword v87, v19, s[44:47], s59 offen\n" \
  "s_add_u32 s59, s74, 0x88000\n" \
  "buffer_load_dword v88, v19, s[44:47], s59 offen\n" \
  "s_add_u32 s59, s74, 0x8a000\n" \
  "buffer_load_dword v89, v19, s[44:47], s59 offen\n" \
  "s_add_u32 s59, s74, 0x8c000\n" \
  "buffer_load_dword v90, v19, s[44:47], s59 offen\n" \
  "s_add_u32 s59, s74, 0x8e000\n" \
  "buffer_load_dword v91, v19, s[44:47], s59 offen\n" \
  "s_add_u32 s59, s74, 0x98000\n" \
  "buffer_load_dword v92, v19, s[44:47], s59 offen\n" \
  "s_add_u32 s59, s74, 0x9a000\n" \
  "buffer_load_dword v93, v19, s[44:47], s59 offen\n" \
  "s_add_u32 s59, s74, 0x9c000\n" \
  "buffer_load_dword v94, v19, s[44:47], s59 offen\n" \
  "s_add_u32 s59, s74, 0x9e000\n" \
  "buffer_load_dword v95, v19, s[44:47], s59 offen\n" \
  "s_waitcnt vmcnt(63)\n" \
  "v_perm_b32 v168, v113, v112, s68\n" \
  "v_perm_b32 v232, v113, v112, s69\n" \
  "v_perm_b32 v169, v115, v114, s68\n" \
  "v_perm_b32 v233, v115, v114, s69\n" \
  "v_perm_b32 v170, v117, v116, s68\n" \
  "v_perm_b32 v234, v117, v116, s69\n" \
  "v_perm_b32 v171, v119, v118, s68\n" \
  "v_perm_b32 v235, v119, v118, s69\n" \
  "v_perm_b32 v172, v121, v120, s68\n" \
  "v_perm_b32 v236, v121, v120, s69\n" \
  "v_perm_b32 v173, v123, v122, s68\n" \
  "v_perm_b32 v237, v123, v122, s69\n" \
  "v_perm_b32 v174, v125, v124, s68\n" \
  "v_perm_b32 v238, v125, v124, s69\n" \
  "v_perm_b32 v175, v127, v126, s68\n" \
  "v_perm_b32 v239, v127, v126, s69\n" \
  "s_add_u32 s59, s75, 0x0\n" \
  "buffer_load_dword v96, v19, s[44:47], s59 offen\n" \
  "s_add_u32 s59, s75, 0x2000\n" \
  "buffer_load_dword v97, v19, s[44:47], s59 offen\n" \
  "s_add_u32 s59, s75, 0x4000\n" \
  "buffer_load_dword v98, v19, s[44:47], s59 offen\n" \
  "s_add_u32 s59, s75, 0x6000\n" \
  "buffer_load_dword v99, v19, s[44:47], s59 offen\n" \
  "s_add_u32 s59, s75, 0x10000\n" \
  "buffer_load_dword v100, v19, s[44:47], s59 offen\n" \
  "s_add_u32 s59, s75, 0x12000\n" \
  "buffer_load_dword v101, v19, s[44:47], s59 offen\n" \
  "s_add_u32 s59, s75, 0x14000\n" \
  "buffer_load_dword v102, v19, s[44:47], s59 offen\n" \
  "s_add_u32 s59, s75, 0x16000\n" \
  "buffer_load_dword v103, v19, s[44:47], s59 offen\n" \
  "s_add_u32 s59, s75, 0x80000\n" \
  "buffer_load_dword v104, v19, s[44:47], s59 offen\n" \
  "s_add_u32 s59, s75, 0x82000\n" \
  "buffer_load_dword v105, v19, s[44:47], s59 offen\n" \
  "s_add_u32 s59, s75, 0x84000\n" \
  "buffer_load_dword v106, v19, s[44:47], s59 offen\n" \
  "s_add_u32 s59, s75, 0x86000\n" \
  "buffer_load_dword v107, v19, s[44:47], s59 offen\n" \
  "s_add_u32 s59, s75, 0x90000\n" \
  "buffer_load_dword v108, v19, s[44:47], s59 offen\n" \
  "s_add_u32 s59, s75, 0x92000\n" \
  "buffer_load_dword v109, v19, s[44:47], s59 offen\n" \
  "s_add_u32 s59, s75, 0x94000\n" \
  "buffer_load_dword v110, v19, s[44:47], s59 offen\n" \
  "s_add_u32 s59, s75, 0x96000\n" \
  "buffer_load_dword v111, v19, s[44:47], s59 offen\n" \
  "s_waitcnt vmcnt(63)\n" \
  "v_perm_b32 v176, v33, v32, s68\n" \
  "v_perm_b32 v240, v33, v32, s69\n" \
  "v_perm_b32 v177, v35, v34, s68\n" \
  "v_perm_b32 v241, v35, v34, s69\n" \
  "v_perm_b32 v178, v37, v36, s68\n" \
  "v_perm_b32 v242, v37, v36, s69\n" \
  "v_perm_b32 v179, v39, v38, s68\n" \
  "v_perm_b32 v243, v39, v38, s69\n" \
  "v_perm_b32 v180, v41, v40, s68\n" \
  "v_perm_b32 v244, v41, v40, s69\n" \
  "v_perm_b32 v181, v43, v42, s68\n" \
  "v_perm_b32 v245, v43, v42, s69\n" \
  "v_perm_b32 v182, v45, v44, s68\n" \
  "v_perm_b32 v246, v45, v44, s69\n" \
  "v_perm_b32 v183, v47, v46, s68\n" \
  "v_perm_b32 v247, v47, v46, s69\n" \
  "s_add_u32 s59, s75, 0x8000\n" \
  "buffer_load_dword v112, v19, s[44:47], s59 offen\n" \
  "s_add_u32 s59, s75, 0xa000\n" \
  "buffer_load_dword v113, v19, s[44:47], s59 offen\n" \
  "s_add_u32 s59, s75, 0xc000\n" \
  "buffer_load_dword v114, v19, s[44:47], s59 offen\n" \
  "s_add_u32 s59, s75, 0xe000\n" \
  "buffer_load_dword v115, v19, s[44:47], s59 offen\n" \
  "s_add_u32 s59, s75, 0x18000\n" \
  "buffer_load_dword v116, v19, s[44:47], s59 offen\n" \
  "s_add_u32 s59, s75, 0x1a000\n" \
  "buffer_load_dword v117, v19, s[44:47], s59 offen\n" \
  "s_add_u32 s59, s75, 0x1c000\n" \
  "buffer_load_dword v118, v19, s[44:47], s59 offen\n" \
  "s_add_u32 s59, s75, 0x1e000\n" \
  "buffer_load_dword v119, v19, s[44:47], s59 offen\n" \
  "s_add_u32 s59, s75, 0x88000\n" \
  "buffer_load_dword v120, v19, s[44:47], s59 offen\n" \
  "s_add_u32 s59, s75, 0x8a000\n" \
  "buffer_load_dword v121, v19, s[44:47], s59 offen\n" \
  "s_add_u32 s59, s75, 0x8c000\n" \
  "buffer_load_dword v122, v19, s[44:47], s59 offen\n" \
  "s_add_u32 s59, s75, 0x8e000\n" \
  "buffer_load_dword v123, v19, s[44:47], s59 offen\n" \
  "s_add_u32 s59, s75, 0x98000\n" \
  "buffer_load_dword v124, v19, s[44:47], s59 offen\n" \
  "s_add_u32 s59, s75, 0x9a000\n" \
  "buffer_load_dword v125, v19, s[44:47], s59 offen\n" \
  "s_add_u32 s59, s75, 0x9c000\n" \
  "buffer_load_dword v126, v19, s[44:47], s59 offen\n" \
  "s_add_u32 s59, s75, 0x9e000\n" \
  "buffer_load_dword v127, v19, s[44:47], s59 offen\n" \
  "s_waitcnt vmcnt(63)\n" \
  "v_perm_b32 v184, v49, v48, s68\n" \
  "v_perm_b32 v248, v49, v48, s69\n" \
  "v_perm_b32 v185, v51, v50, s68\n" \
  "v_perm_b32 v249, v51, v50, s69\n" \
  "v_perm_b32 v186, v53, v52, s68\n" \
  "v_perm_b32 v250, v53, v52, s69\n" \
  "v_perm_b32 v187, v55, v54, s68\n" \
  "v_perm_b32 v251, v55, v54, s69\n" \
  "v_perm_b32 v188, v57, v56, s68\n" \
  "v_perm_b32 v252, v57, v56, s69\n" \
  "v_perm_b32 v189, v59, v58, s68\n" \
  "v_perm_b32 v253, v59, v58, s69\n" \
  "v_perm_b32 v190, v61, v60, s68\n" \
  "v_perm_b32 v254, v61, v60, s69\n" \
  "v_perm_b32 v191, v63, v62, s68\n" \
  "v_perm_b32 v255, v63, v62, s69\n" \
  "s_add_u32 s59, s76, 0x0\n" \
  "buffer_load_dword v32, v19, s[44:47], s59 offen\n" \
  "s_add_u32 s59, s76, 0x2000\n" \
  "buffer_load_dword v33, v19, s[44:47], s59 offen\n" \
  "s_add_u32 s59, s76, 0x4000\n" \
  "buffer_load_dword v34, v19, s[44:47], s59 offen\n" \
  "s_add_u32 s59, s76, 0x6000\n" \
  "buffer_load_dword v35, v19, s[44:47], s59 offen\n" \
  "s_add_u32 s59, s76, 0x10000\n" \
  "buffer_load_dword v36, v19, s[44:47], s59 offen\n" \
  "s_add_u32 s59, s76, 0x12000\n" \
  "buffer_load_dword v37, v19, s[44:47], s59 offen\n" \
  "s_add_u32 s59, s76, 0x14000\n" \
  "buffer_load_dword v38, v19, s[44:47], s59 offen\n" \
  "s_add_u32 s59, s76, 0x16000\n" \
  "buffer_load_dword v39, v19, s[44:47], s59 offen\n" \
  "s_add_u32 s59, s76, 0x80000\n" \
  "buffer_load_dword v40, v19, s[44:47], s59 offen\n" \
  "s_add_u32 s59, s76, 0x82000\n" \
  "buffer_load_dword v41, v19, s[44:47], s59 offen\n" \
  "s_add_u32 s59, s76, 0x84000\n" \
  "buffer_load_dword v42, v19, s[44:47], s59 offen\n" \
  "s_add_u32 s59, s76, 0x86000\n" \
  "buffer_load_dword v43, v19, s[44:47], s59 offen\n" \
  "s_add_u32 s59, s76, 0x90000\n" \
  "buffer_load_dword v44, v19, s[44:47], s59 offen\n" \
  "s_add_u32 s59, s76, 0x92000\n" \
  "buffer_load_dword v45, v19, s[44:47], s59 offen\n" \
  "s_add_u32 s59, s76, 0x94000\n" \
  "buffer_load_dword v46, v19, s[44:47], s59 offen\n" \
  "s_add_u32 s59, s76, 0x96000\n" \
  "buffer_load_dword v47, v19, s[44:47], s59 offen\n" \
  "s_waitcnt vmcnt(63)\n" \
  "v_perm_b32 v20, v65, v64, s68\n" \
  "v_perm_b32 v21, v65, v64, s69\n" \
  "v_accvgpr_write_b32 a0, v20\n" \
  "v_accvgpr_write_b32 a64, v21\n" \
  "v_perm_b32 v20, v67, v66, s68\n" \
  "v_perm_b32 v21, v67, v66, s69\n" \
  "v_accvgpr_write_b32 a1, v20\n" \
  "v_accvgpr_write_b32 a65, v21\n" \
  "v_perm_b32 v20, v69, v68, s68\n" \
  "v_perm_b32 v21, v69, v68, s69\n" \
  "v_accvgpr_write_b32 a2, v20\n" \
  "v_accvgpr_write_b32 a66, v21\n" \
  "v_perm_b32 v20, v71, v70, s68\n" \
  "v_perm_b32 v21, v71, v70, s69\n" \
  "v_accvgpr_write_b32 a3, v20\n" \
  "v_accvgpr_write_b32 a67, v21\n" \
  "v_perm_b32 v20, v73, v72, s68\n" \
  "v_perm_b32 v21, v73, v72, s69\n" \
  "v_accvgpr_write_b32 a4, v20\n" \
  "v_accvgpr_write_b32 a68, v21\n" \
  "v_perm_b32 v20, v75, v74, s68\n" \
  "v_perm_b32 v21, v75, v74, s69\n" \
  "v_accvgpr_write_b32 a5, v20\n" \
  "v_accvgpr_write_b32 a69, v21\n" \
  "v_perm_b32 v20, v77, v76, s68\n" \
  "v_perm_b32 v21, v77, v76, s69\n" \
  "v_accvgpr_write_b32 a6, v20\n" \
  "v_accvgpr_write_b32 a70, v21\n" \
  "v_perm_b32 v20, v79, v78, s68\n" \
  "v_perm_b32 v21, v79, v78, s69\n" \
  "v_accvgpr_write_b32 a7, v20\n" \
  "v_accvgpr_write_b32 a71, v21\n" \
  "s_add_u32 s59, s76, 0x8000\n" \
  "buffer_load_dword v48, v19, s[44:47], s59 offen\n" \
  "s_add_u32 s59, s76, 0xa000\n" \
  "buffer_load_dword v49, v19, s[44:47], s59 offen\n" \
  "s_add_u32 s59, s76, 0xc000\n" \
  "buffer_load_dword v50, v19, s[44:47], s59 offen\n" \
  "s_add_u32 s59, s76, 0xe000\n" \
  "buffer_load_dword v51, v19, s[44:47], s59 offen\n" \
  "s_add_u32 s59, s76, 0x18000\n" \
  "buffer_load_dword v52, v19, s[44:47], s59 offen\n" \
  "s_add_u32 s59, s76, 0x1a000\n" \
  "buffer_load_dword v53, v19, s[44:47], s59 offen\n" \
  "s_add_u32 s59, s76, 0x1c000\n" \
  "buffer_load_dword v54, v19, s[44:47], s59 offen\n" \
  "s_add_u32 s59, s76, 0x1e000\n" \
  "buffer_load_dword v55, v19, s[44:47], s59 offen\n" \
  "s_add_u32 s59, s76, 0x88000\n" \
  "buffer_load_dword v56, v19, s[44:47], s59 offen\n" \
  "s_add_u32 s59, s76, 0x8a000\n" \
  "buffer_load_dword v57, v19, s[44:47], s59 offen\n" \
  "s_add_u32 s59, s76, 0x8c000\n" \
  "buffer_load_dword v58, v19, s[44:47], s59 offen\n" \
  "s_add_u32 s59, s76, 0x8e000\n" \
  "buffer_load_dword v59, v19, s[44:47], s59 offen\n" \
  "s_add_u32 s59, s76, 0x98000\n" \
  "buffer_load_dword v60, v19, s[44:47], s59 offen\n" \
  "s_add_u32 s59, s76, 0x9a000\n" \
  "buffer_load_dword v61, v19, s[44:47], s59 offen\n" \
  "s_add_u32 s59, s76, 0x9c000\n" \
  "buffer_load_dword v62, v19, s[44:47], s59 offen\n" \
  "s_add_u32 s59, s76, 0x9e000\n" \
  "buffer_load_dword v63, v19, s[44:47], s59 offen\n" \
  "s_waitcnt vmcnt(63)\n" \
  "v_perm_b32 v20, v81, v80, s68\n" \
  "v_perm_b32 v21, v81, v80, s69\n" \
  "v_accvgpr_write_b32 a8, v20\n" \
  "v_accvgpr_write_b32 a72, v21\n" \
  "v_perm_b32 v20, v83, v82, s68\n" \
  "v_perm_b32 v21, v83, v82, s69\n" \
  "v_accvgpr_write_b32 a9, v20\n" \
  "v_accvgpr_write_b32 a73, v21\n" \
  "v_perm_b32 v20, v85, v84, s68\n" \
  "v_perm_b32 v21, v85, v84, s69\n" \
  "v_accvgpr_write_b32 a10, v20\n" \
  "v_accvgpr_write_b32 a74, v21\n" \
  "v_perm_b32 v20, v87, v86, s68\n" \
  "v_perm_b32 v21, v87, v86, s69\n" \
  "v_accvgpr_write_b32 a11, v20\n" \
  "v_accvgpr_write_b32 a75, v21\n" \
  "v_perm_b32 v20, v89, v88, s68\n" \
  "v_perm_b32 v21, v89, v88, s69\n" \
  "v_accvgpr_write_b32 a12, v20\n" \
  "v_accvgpr_write_b32 a76, v21\n" \
  "v_perm_b32 v20, v91, v90, s68\n" \
  "v_perm_b32 v21, v91, v90, s69\n" \
  "v_accvgpr_write_b32 a13, v20\n" \
  "v_accvgpr_write_b32 a77, v21\n" \
  "v_perm_b32 v20, v93, v92, s68\n" \
  "v_perm_b32 v21, v93, v92, s69\n" \
  "v_accvgpr_write_b32 a14, v20\n" \
  "v_accvgpr_write_b32 a78, v21\n" \
  "v_perm_b32 v20, v95, v94, s68\n" \
  "v_perm_b32 v21, v95, v94, s69\n" \
  "v_accvgpr_write_b32 a15, v20\n" \
  "v_accvgpr_write_b32 a79, v21\n" \
  "s_add_u32 s59, s77, 0x0\n" \
  "buffer_load_dword v64, v19, s[44:47], s59 offen\n" \
  "s_add_u32 s59, s77, 0x2000\n" \
  "buffer_load_dword v65, v19, s[44:47], s59 offen\n" \
  "s_add_u32 s59, s77, 0x4000\n" \
  "buffer_load_dword v66, v19, s[44:47], s59 offen\n" \
  "s_add_u32 s59, s77, 0x6000\n" \
  "buffer_load_dword v67, v19, s[44:47], s59 offen\n" \
  "s_add_u32 s59, s77, 0x10000\n" \
  "buffer_load_dword v68, v19, s[44:47], s59 offen\n" \
  "s_add_u32 s59, s77, 0x12000\n" \
  "buffer_load_dword v69, v19, s[44:47], s59 offen\n" \
  "s_add_u32 s59, s77, 0x14000\n" \
  "buffer_load_dword v70, v19, s[44:47], s59 offen\n" \
  "s_add_u32 s59, s77, 0x16000\n" \
  "buffer_load_dword v71, v19, s[44:47], s59 offen\n" \
  "s_add_u32 s59, s77, 0x80000\n" \
  "buffer_load_dword v72, v19, s[44:47], s59 offen\n" \
  "s_add_u32 s59, s77, 0x82000\n" \
  "buffer_load_dword v73, v19, s[44:47], s59 offen\n" \
  "s_add_u32 s59, s77, 0x84000\n" \
  "buffer_load_dword v74, v19, s[44:47], s59 offen\n" \
  "s_add_u32 s59, s77, 0x86000\n" \
  "buffer_load_dword v75, v19, s[44:47], s59 offen\n" \
  "s_add_u32 s59, s77, 0x90000\n" \
  "buffer_load_dword v76, v19, s[44:47], s59 offen\n" \
  "s_add_u32 s59, s77, 0x92000\n" \
  "buffer_load_dword v77, v19, s[44:47], s59 offen\n" \
  "s_add_u32 s59, s77, 0x94000\n" \
  "buffer_load_dword v78, v19, s[44:47], s59 offen\n" \
  "s_add_u32 s59, s77, 0x96000\n" \
  "buffer_load_dword v79, v19, s[44:47], s59 offen\n" \
  "s_waitcnt vmcnt(63)\n" \
  "v_perm_b32 v20, v97, v96, s68\n" \
  "v_perm_b32 v21, v97, v96, s69\n" \
  "v_accvgpr_write_b32 a16, v20\n" \
  "v_accvgpr_write_b32 a80, v21\n" \
  "v_perm_b32 v20, v99, v98, s68\n" \
  "v_perm_b32 v21, v99, v98, s69\n" \
  "v_accvgpr_write_b32 a17, v20\n" \
  "v_accvgpr_write_b32 a81, v21\n" \
  "v_perm_b32 v20, v101, v100, s68\n" \
  "v_perm_b32 v21, v101, v100, s69\n" \
  "v_accvgpr_write_b32 a18, v20\n" \
  "v_accvgpr_write_b32 a82, v21\n" \
  "v_perm_b32 v20, v103, v102, s68\n" \
  "v_perm_b32 v21, v103, v102, s69\n" \
  "v_accvgpr_write_b32 a19, v20\n" \
  "v_accvgpr_write_b32 a83, v21\n" \
  "v_perm_b32 v20, v105, v104, s68\n" \
  "v_perm_b32 v21, v105, v104, s69\n" \
  "v_accvgpr_write_b32 a20, v20\n" \
  "v_accvgpr_write_b32 a84, v21\n" \
  "v_perm_b32 v20, v107, v106, s68\n" \
  "v_perm_b32 v21, v107, v106, s69\n" \
  "v_accvgpr_write_b32 a21, v20\n" \
  "v_accvgpr_write_b32 a85, v21\n" \
  "v_perm_b32 v20, v109, v108, s68\n" \
  "v_perm_b32 v21, v109, v108, s69\n" \
  "v_accvgpr_write_b32 a22, v20\n" \
  "v_accvgpr_write_b32 a86, v21\n" \
  "v_perm_b32 v20, v111, v110, s68\n" \
  "v_perm_b32 v21, v111, v110, s69\n" \
  "v_accvgpr_write_b32 a23, v20\n" \
  "v_accvgpr_write_b32 a87, v21\n" \
  "s_add_u32 s59, s77, 0x8000\n" \
  "buffer_load_dword v80, v19, s[44:47], s59 offen\n" \
  "s_add_u32 s59, s77, 0xa000\n" \
  "buffer_load_dword v81, v19, s[44:47], s59 offen\n" \
  "s_add_u32 s59, s77, 0xc000\n" \
  "buffer_load_dword v82, v19, s[44:47], s59 offen\n" \
  "s_add_u32 s59, s77, 0xe000\n" \
  "buffer_load_dword v83, v19, s[44:47], s59 offen\n" \
  "s_add_u32 s59, s77, 0x18000\n" \
  "buffer_load_dword v84, v19, s[44:47], s59 offen\n" \
  "s_add_u32 s59, s77, 0x1a000\n" \
  "buffer_load_dword v85, v19, s[44:47], s59 offen\n" \
  "s_add_u32 s59, s77, 0x1c000\n" \
  "buffer_load_dword v86, v19, s[44:47], s59 offen\n" \
  "s_add_u32 s59, s77, 0x1e000\n" \
  "buffer_load_dword v87, v19, s[44:47], s59 offen\n" \
  "s_add_u32 s59, s77, 0x88000\n" \
  "buffer_load_dword v88, v19, s[44:47], s59 offen\n" \
  "s_add_u32 s59, s77, 0x8a000\n" \
  "buffer_load_dword v89, v19, s[44:47], s59 offen\n" \
  "s_add_u32 s59, s77, 0x8c000\n" \
  "buffer_load_dword v90, v19, s[44:47], s59 offen\n" \
  "s_add_u32 s59, s77, 0x8e000\n" \
  "buffer_load_dword v91, v19, s[44:47], s59 offen\n" \
  "s_add_u32 s59, s77, 0x98000\n" \
  "buffer_load_dword v92, v19, s[44:47], s59 offen\n" \
  "s_add_u32 s59, s77, 0x9a000\n" \
  "buffer_load_dword v93, v19, s[44:47], s59 offen\n" \
  "s_add_u32 s59, s77, 0x9c000\n" \
  "buffer_load_dword v94, v19, s[44:47], s59 offen\n" \
  "s_add_u32 s59, s77, 0x9e000\n" \
  "buffer_load_dword v95, v19, s[44:47], s59 offen\n" \
  "s_waitcnt vmcnt(63)\n" \
  "v_perm_b32 v20, v113, v112, s68\n" \
  "v_perm_b32 v21, v113, v112, s69\n" \
  "v_accvgpr_write_b32 a24, v20\n" \
  "v_accvgpr_write_b32 a88, v21\n" \
  "v_perm_b32 v20, v115, v114, s68\n" \
  "v_perm_b32 v21, v115, v114, s69\n" \
  "v_accvgpr_write_b32 a25, v20\n" \
  "v_accvgpr_write_b32 a89, v21\n" \
  "v_perm_b32 v20, v117, v116, s68\n" \
  "v_perm_b32 v21, v117, v116, s69\n" \
  "v_accvgpr_write_b32 a26, v20\n" \
  "v_accvgpr_write_b32 a90, v21\n" \
  "v_perm_b32 v20, v119, v118, s68\n" \
  "v_perm_b32 v21, v119, v118, s69\n" \
  "v_accvgpr_write_b32 a27, v20\n" \
  "v_accvgpr_write_b32 a91, v21\n" \
  "v_perm_b32 v20, v121, v120, s68\n" \
  "v_perm_b32 v21, v121, v120, s69\n" \
  "v_accvgpr_write_b32 a28, v20\n" \
  "v_accvgpr_write_b32 a92, v21\n" \
  "v_perm_b32 v20, v123, v122, s68\n" \
  "v_perm_b32 v21, v123, v122, s69\n" \
  "v_accvgpr_write_b32 a29, v20\n" \
  "v_accvgpr_write_b32 a93, v21\n" \
  "v_perm_b32 v20, v125, v124, s68\n" \
  "v_perm_b32 v21, v125, v124, s69\n" \
  "v_accvgpr_write_b32 a30, v20\n" \
  "v_accvgpr_write_b32 a94, v21\n" \
  "v_perm_b32 v20, v127, v126, s68\n" \
  "v_perm_b32 v21, v127, v126, s69\n" \
  "v_accvgpr_write_b32 a31, v20\n" \
  "v_accvgpr_write_b32 a95, v21\n" \
  "s_waitcnt vmcnt(62)\n" \
  "v_perm_b32 v20, v33, v32, s68\n" \
  "v_perm_b32 v21, v33, v32, s69\n" \
  "v_accvgpr_write_b32 a32, v20\n" \
  "v_accvgpr_write_b32 a96, v21\n" \
  "s_waitcnt vmcnt(60)\n" \
  "v_perm_b32 v20, v35, v34, s68\n" \
  "v_perm_b32 v21, v35, v34, s69\n" \
  "v_accvgpr_write_b32 a33, v20\n" \
  "v_accvgpr_write_b32 a97, v21\n" \
  "s_waitcnt vmcnt(58)\n" \
  "v_perm_b32 v20, v37, v36, s68\n" \
  "v_perm_b32 v21, v37, v36, s69\n" \
  "v_accvgpr_write_b32 a34, v20\n" \
  "v_accvgpr_write_b32 a98, v21\n" \
  "s_waitcnt vmcnt(56)\n" \
  "v_perm_b32 v20, v39, v38, s68\n" \
  "v_perm_b32 v21, v39, v38, s69\n" \
  "v_accvgpr_write_b32 a35, v20\n" \
  "v_accvgpr_write_b32 a99, v21\n" \
  "s_waitcnt vmcnt(54)\n" \
  "v_perm_b32 v20, v41, v40, s68\n" \
  "v_perm_b32 v21, v41, v40, s69\n" \
  "v_accvgpr_write_b32 a36, v20\n" \
  "v_accvgpr_write_b32 a100, v21\n" \
  "s_waitcnt vmcnt(52)\n" \
  "v_perm_b32 v20, v43, v42, s68\n" \
  "v_perm_b32 v21, v43, v42, s69\n" \
  "v_accvgpr_write_b32 a37, v20\n" \
  "v_accvgpr_write_b32 a101, v21\n" \
  "s_waitcnt vmcnt(50)\n" \
  "v_perm_b32 v20, v45, v44, s68\n" \
  "v_perm_b32 v21, v45, v44, s69\n" \
  "v_accvgpr_write_b32 a38, v20\n" \
  "v_accvgpr_write_b32 a102, v21\n" \
  "s_waitcnt vmcnt(48)\n" \
  "v_perm_b32 v20, v47, v46, s68\n" \
  "v_perm_b32 v21, v47, v46, s69\n" \
  "v_accvgpr_write_b32 a39, v20\n" \
  "v_accvgpr_write_b32 a103, v21\n" \
  "s_waitcnt vmcnt(46)\n" \
  "v_perm_b32 v20, v49, v48, s68\n" \
  "v_perm_b32 v21, v49, v48, s69\n" \
  "v_accvgpr_write_b32 a40, v20\n" \
  "v_accvgpr_write_b32 a104, v21\n" \
  "s_waitcnt vmcnt(44)\n" \
  "v_perm_b32 v20, v51, v50, s68\n" \
  "v_perm_b32 v21, v51, v50, s69\n" \
  "v_accvgpr_write_b32 a41, v20\n" \
  "v_accvgpr_write_b32 a105, v21\n" \
  "s_waitcnt vmcnt(42)\n" \
  "v_perm_b32 v20, v53, v52, s68\n" \
  "v_perm_b32 v21, v53, v52, s69\n" \
  "v_accvgpr_write_b32 a42, v20\n" \
  "v_accvgpr_write_b32 a106, v21\n" \
  "s_waitcnt vmcnt(40)\n" \
  "v_perm_b32 v20, v55, v54, s68\n" \
  "v_perm_b32 v21, v55, v54, s69\n" \
  "v_accvgpr_write_b32 a43, v20\n" \
  "v_accvgpr_write_b32 a107, v21\n" \
  "s_waitcnt vmcnt(38)\n" \
  "v_perm_b32 v20, v57, v56, s68\n" \
  "v_perm_b32 v21, v57, v56, s69\n" \
  "v_accvgpr_write_b32 a44, v20\n" \
  "v_accvgpr_write_b32 a108, v21\n" \
  "s_waitcnt vmcnt(36)\n" \
  "v_perm_b32 v20, v59, v58, s68\n" \
  "v_perm_b32 v21, v59, v58, s69\n" \
  "v_accvgpr_write_b32 a45, v20\n" \
  "v_accvgpr_write_b32 a109, v21\n" \
  "s_waitcnt vmcnt(34)\n" \
  "v_perm_b32 v20, v61, v60, s68\n" \
  "v_perm_b32 v21, v61, v60, s69\n" \
  "v_accvgpr_write_b32 a46, v20\n" \
  "v_accvgpr_write_b32 a110, v21\n" \
  "s_waitcnt vmcnt(32)\n" \
  "v_perm_b32 v20, v63, v62, s68\n" \
  "v_perm_b32 v21, v63, v62, s69\n" \
  "v_accvgpr_write_b32 a47, v20\n" \
  "v_accvgpr_write_b32 a111, v21\n" \
  "s_waitcnt vmcnt(30)\n" \
  "v_perm_b32 v20, v65, v64, s68\n" \
  "v_perm_b32 v21, v65, v64, s69\n" \
  "v_accvgpr_write_b32 a48, v20\n" \
  "v_accvgpr_write_b32 a112, v21\n" \
  "s_waitcnt vmcnt(28)\n" \
  "v_perm_b32 v20, v67, v66, s68\n" \
  "v_perm_b32 v21, v67, v66, s69\n" \
  "v_accvgpr_write_b32 a49, v20\n" \
  "v_accvgpr_write_b32 a113, v21\n" \
  "s_waitcnt vmcnt(26)\n" \
  "v_perm_b32 v20, v69, v68, s68\n" \
  "v_perm_b32 v21, v69, v68, s69\n" \
  "v_accvgpr_write_b32 a50, v20\n" \
  "v_accvgpr_write_b32 a114, v21\n" \
  "s_waitcnt vmcnt(24)\n" \
  "v_perm_b32 v20, v71, v70, s68\n" \
  "v_perm_b32 v21, v71, v70, s69\n" \
  "v_accvgpr_write_b32 a51, v20\n" \
  "v_accvgpr_write_b32 a115, v21\n" \
  "s_waitcnt vmcnt(22)\n" \
  "v_perm_b32 v20, v73, v72, s68\n" \
  "v_perm_b32 v21, v73, v72, s69\n" \
  "v_accvgpr_write_b32 a52, v20\n" \
  "v_accvgpr_write_b32 a116, v21\n" \
  "s_waitcnt vmcnt(20)\n" \
  "v_perm_b32 v20, v75, v74, s68\n" \
  "v_perm_b32 v21, v75, v74, s69\n" \
  "v_accvgpr_write_b32 a53, v20\n" \
  "v_accvgpr_write_b32 a117, v21\n" \
  "s_waitcnt vmcnt(18)\n" \
  "v_perm_b32 v20, v77, v76, s68\n" \
  "v_perm_b32 v21, v77, v76, s69\n" \
  "v_accvgpr_write_b32 a54, v20\n" \
  "v_accvgpr_write_b32 a118, v21\n" \
  "s_waitcnt vmcnt(16)\n" \
  "v_perm_b32 v20, v79, v78, s68\n" \
  "v_perm_b32 v21, v79, v78, s69\n" \
  "v_accvgpr_write_b32 a55, v20\n" \
  "v_accvgpr_write_b32 a119, v21\n" \
  "s_waitcnt vmcnt(14)\n" \
  "v_perm_b32 v20, v81, v80, s68\n" \
  "v_perm_b32 v21, v81, v80, s69\n" \
  "v_accvgpr_write_b32 a56, v20\n" \
  "v_accvgpr_write_b32 a120, v21\n" \
  "s_waitcnt vmcnt(12)\n" \
  "v_perm_b32 v20, v83, v82, s68\n" \
  "v_perm_b32 v21, v83, v82, s69\n" \
  "v_accvgpr_write_b32 a57, v20\n" \
  "v_accvgpr_write_b32 a121, v21\n" \
  "s_waitcnt vmcnt(10)\n" \
  "v_perm_b32 v20, v85, v84, s68\n" \
  "v_perm_b32 v21, v85, v84, s69\n" \
  "v_accvgpr_write_b32 a58, v20\n" \
  "v_accvgpr_write_b32 a122, v21\n" \
  "s_waitcnt vmcnt(8)\n" \
  "v_perm_b32 v20, v87, v86, s68\n" \
  "v_perm_b32 v21, v87, v86, s69\n" \
  "v_accvgpr_write_b32 a59, v20\n" \
  "v_accvgpr_write_b32 a123, v21\n" \
  "s_waitcnt vmcnt(6)\n" \
  "v_perm_b32 v20, v89, v88, s68\n" \
  "v_perm_b32 v21, v89, v88, s69\n" \
  "v_accvgpr_write_b32 a60, v20\n" \
  "v_accvgpr_write_b32 a124, v21\n" \
  "s_waitcnt vmcnt(4)\n" \
  "v_perm_b32 v20, v91, v90, s68\n" \
  "v_perm_b32 v21, v91, v90, s69\n" \
  "v_accvgpr_write_b32 a61, v20\n" \
  "v_accvgpr_write_b32 a125, v21\n" \
  "s_waitcnt vmcnt(2)\n" \
  "v_perm_b32 v20, v93, v92, s68\n" \
  "v_perm_b32 v21, v93, v92, s69\n" \
  "v_accvgpr_write_b32 a62, v20\n" \
  "v_accvgpr_write_b32 a126, v21\n" \
  "s_waitcnt vmcnt(0)\n" \
  "v_perm_b32 v20, v95, v94, s68\n" \
  "v_perm_b32 v21, v95, v94, s69\n" \
  "v_accvgpr_write_b32 a63, v20\n" \
  "v_accvgpr_write_b32 a127, v21\n" \
  "s_waitcnt vmcnt(0) lgkmcnt(0)\n" \
  "VLOOP_%=:\n" \
  "buffer_load_dwordx4 v[16:19], v0, s[40:43], s49 offen sc1\n" \
  "buffer_load_dwordx4 v[20:23], v0, s[40:43], s49 offen offset:1024 sc1\n" \
  "s_mov_b32 s61, 0\n" \
  "v_add_u32 v120, s84, v0\n" \
  "v_mov_b64 v[48:49], 0\n" \
  "v_mov_b64 v[50:51], 0\n" \
  "v_mov_b64 v[52:53], 0\n" \
  "v_mov_b64 v[54:55], 0\n" \
  "v_mov_b64 v[56:57], 0\n" \
  "v_mov_b64 v[58:59], 0\n" \
  "v_mov_b64 v[60:61], 0\n" \
  "v_mov_b64 v[62:63], 0\n" \
  "s_and_b32 s60, s48, 1\n" \
  "s_cmp_eq_u32 s60, 0\n" \
  "s_cselect_b32 s72, s79, s73\n" \
  "s_add_u32 s70, s48, 1\n" \
  ".rept %c[wg]\n" \
  "s_nop 7\n" \
  ".endr\n" \
  "buffer_load_dwordx4 v[96:99], v0, s[40:43], s49 offen sc1\n" \
  "buffer_load_dwordx4 v[100:103], v0, s[40:43], s49 offen offset:1024 sc1\n" \
  "s_nop 7\n" \
  ".rept %c[wg]\n" \
  "s_nop 7\n" \
  ".endr\n" \
  "buffer_load_dwordx4 v[104:107], v0, s[40:43], s49 offen sc1\n" \
  "buffer_load_dwordx4 v[108:111], v0, s[40:43], s49 offen offset:1024 sc1\n" \
  "s_waitcnt vmcnt(5)\n" \
  "v_max3_u32 v47, v16, v17, v18\n" \
  "s_waitcnt vmcnt(4)\n" \
  "v_max3_u32 v47, v47, v19, v20\n" \
  "v_max3_u32 v47, v47, v21, v22\n" \
  "v_max_u32 v47, v47, v23\n" \
  "v_cmp_eq_u32 vcc, -1, v47\n" \
  "s_cbranch_vccnz VMB_MISS_%=\n" \
  "VMB_OK_%=:\n" \
  "ds_write_b128 v120, v[16:19]\n" \
  "ds_write_b128 v120, v[20:23] offset:1024\n" \
  "v_smfmac_f32_16x16x64_bf16 v[48:51], v[16:19], v[128:135], v1\n" \
  "v_smfmac_f32_16x16x64_bf16 v[52:55], v[16:19], v[192:199], v1\n" \
  "v_smfmac_f32_16x16x64_bf16 v[48:51], v[20:23], v[136:143], v1\n" \
  "v_smfmac_f32_16x16x64_bf16 v[52:55], v[20:23], v[200:207], v1\n" \
  "s_waitcnt lgkmcnt(0)\n" \
  "s_barrier\n" \
  "ds_read_b128 v[24:27], v13\n" \
  "ds_read_b128 v[28:31], v13 offset:1024\n" \
  "ds_read_b128 v[32:35], v14\n" \
  "ds_read_b128 v[36:39], v14 offset:1024\n" \
  "ds_read_b128 v[40:43], v15\n" \
  "ds_read_b128 v[44:47], v15 offset:1024\n" \
  "s_waitcnt lgkmcnt(5)\n" \
  "v_smfmac_f32_16x16x64_bf16 v[48:51], v[24:27], v[144:151], v1\n" \
  "v_smfmac_f32_16x16x64_bf16 v[52:55], v[24:27], v[208:215], v1\n" \
  "s_waitcnt lgkmcnt(4)\n" \
  "v_smfmac_f32_16x16x64_bf16 v[48:51], v[28:31], v[152:159], v1\n" \
  "v_smfmac_f32_16x16x64_bf16 v[52:55], v[28:31], v[216:223], v1\n" \
  "s_waitcnt lgkmcnt(3)\n" \
  "v_smfmac_f32_16x16x64_bf16 v[48:51], v[32:35], v[160:167], v1\n" \
  "v_smfmac_f32_16x16x64_bf16 v[52:55], v[32:35], v[224:231], v1\n" \
  "s_waitcnt lgkmcnt(2)\n" \
  "v_smfmac_f32_16x16x64_bf16 v[48:51], v[36:39], v[168:175], v1\n" \
  "v_smfmac_f32_16x16x64_bf16 v[52:55], v[36:39], v[232:239], v1\n" \
  "s_waitcnt lgkmcnt(1)\n" \
  "v_smfmac_f32_16x16x64_bf16 v[48:51], v[40:43], v[176:183], v1\n" \
  "v_smfmac_f32_16x16x64_bf16 v[52:55], v[40:43], v[240:247], v1\n" \
  "s_waitcnt lgkmcnt(0)\n" \
  "v_smfmac_f32_16x16x64_bf16 v[48:51], v[44:47], v[184:191], v1\n" \
  "v_smfmac_f32_16x16x64_bf16 v[52:55], v[44:47], v[248:255], v1\n" \
  "v_mov_b32 v69, s70\n" \
  "v_mov_b32 v71, s70\n" \
  "v_smfmac_f32_16x16x64_bf16 v[56:59], v[16:19], a[0:7], v1\n" \
  "v_smfmac_f32_16x16x64_bf16 v[60:63], v[16:19], a[64:71], v1\n" \
  "s_nop 2 ; auto\n" \
  "v_pk_add_f32 v[64:65], v[48:49], v[50:51]\n" \
  "v_pk_add_f32 v[66:67], v[52:53], v[54:55]\n" \
  "v_cvt_pk_bf16_f32 v68, v64, v65\n" \
  "v_cvt_pk_bf16_f32 v70, v66, v67\n" \
  "buffer_store_dwordx4 v[68:71], v0, s[40:43], s72 offen sc1\n" \
  "v_smfmac_f32_16x16x64_bf16 v[56:59], v[20:23], a[8:15], v1\n" \
  "v_smfmac_f32_16x16x64_bf16 v[60:63], v[20:23], a[72:79], v1\n" \
  "v_smfmac_f32_16x16x64_bf16 v[56:59], v[24:27], a[16:23], v1\n" \
  "v_smfmac_f32_16x16x64_bf16 v[60:63], v[24:27], a[80:87], v1\n" \
  "v_smfmac_f32_16x16x64_bf16 v[56:59], v[28:31], a[24:31], v1\n" \
  "v_smfmac_f32_16x16x64_bf16 v[60:63], v[28:31], a[88:95], v1\n" \
  "v_smfmac_f32_16x16x64_bf16 v[56:59], v[32:35], a[32:39], v1\n" \
  "v_smfmac_f32_16x16x64_bf16 v[60:63], v[32:35], a[96:103], v1\n" \
  "v_smfmac_f32_16x16x64_bf16 v[56:59], v[36:39], a[40:47], v1\n" \
  "v_smfmac_f32_16x16x64_bf16 v[60:63], v[36:39], a[104:111], v1\n" \
  "v_smfmac_f32_16x16x64_bf16 v[56:59], v[40:43], a[48:55], v1\n" \
  "v_smfmac_f32_16x16x64_bf16 v[60:63], v[40:43], a[112:119], v1\n" \
  "v_smfmac_f32_16x16x64_bf16 v[56:59], v[44:47], a[56:63], v1\n" \
  "v_smfmac_f32_16x16x64_bf16 v[60:63], v[44:47], a[120:127], v1\n" \
  "v_lshrrev_b32 v72, 1, v0\n" \
  "v_mov_b32 v74, v68\n" \
  "v_mov_b32 v75, v70\n" \
  "buffer_store_dwordx2 v[74:75], v72, s[40:43], s56 offen sc1\n" \
  "s_nop 2 ; auto\n" \
  "v_pk_add_f32 v[80:81], v[56:57], v[58:59]\n" \
  "v_pk_add_f32 v[82:83], v[60:61], v[62:63]\n" \
  "v_cvt_pk_bf16_f32 v84, v80, v81\n" \
  "v_cvt_pk_bf16_f32 v85, v82, v83\n" \
  "ds_write2st64_b32 v2, v84, v85 offset0:0 offset1:4\n" \
  "v_xor_b32 v92, s85, v3\n" \
  "v_add_u32 v92, s83, v92\n" \
  "s_waitcnt lgkmcnt(0)\n" \
  "s_barrier\n" \
  "ds_read_b64_tr_b16 v[88:89], v92\n" \
  "ds_read_b64_tr_b16 v[90:91], v92 offset:1024\n" \
  "s_waitcnt lgkmcnt(0)\n" \
  "buffer_store_dwordx4 v[88:91], v0, s[40:43], s55 offen sc1\n" \
  "s_waitcnt vmcnt(0)\n" \
  "s_add_u32 s59, s58, 32\n" \
  "s_add_u32 s59, s59, s67\n" \
  "s_mov_b64 exec, 1\n" \
  "buffer_store_dword v11, off, s[40:43], s59 sc1\n" \
  "s_mov_b64 exec, -1\n" \
  "s_cmp_eq_u32 s48, 63\n" \
  "s_cbranch_scc1 VEND_%=\n" \
  "s_add_u32 s58, s58, 2048\n" \
  "s_add_u32 s56, s56, 65536\n" \
  "s_add_u32 s55, s55, 131072\n" \
  "s_add_u32 s48, s48, 1\n" \
  "s_add_u32 s49, s49, 16384\n" \
  "s_mov_b32 s61, 0\n" \
  "s_cmp_lg_u32 s66, 0\n" \
  "s_cbranch_scc1 VWIT_OK_%=\n" \
  "s_mov_b64 exec, 1\n" \
  "buffer_load_dword v96, off, s[40:43], s78 sc1\n" \
  "buffer_load_dword v97, off, s[40:43], s78 sc1\n" \
  "buffer_load_dword v98, off, s[40:43], s78 sc1\n" \
  "buffer_load_dword v99, off, s[40:43], s78 sc1\n" \
  "VWIT_%=:\n" \
  "s_waitcnt vmcnt(3)\n" \
  "v_cmp_ne_u32 vcc, s48, v96\n" \
  "s_cbranch_vccz VWIT_OK_%=\n" \
  "buffer_load_dword v96, off, s[40:43], s78 sc1\n" \
  "s_waitcnt vmcnt(3)\n" \
  "v_cmp_ne_u32 vcc, s48, v97\n" \
  "s_cbranch_vccz VWIT_OK_%=\n" \
  "buffer_load_dword v97, off, s[40:43], s78 sc1\n" \
  "s_waitcnt vmcnt(3)\n" \
  "v_cmp_ne_u32 vcc, s48, v98\n" \
  "s_cbranch_vccz VWIT_OK_%=\n" \
  "buffer_load_dword v98, off, s[40:43], s78 sc1\n" \
  "s_waitcnt vmcnt(3)\n" \
  "v_cmp_ne_u32 vcc, s48, v99\n" \
  "s_cbranch_vccz VWIT_OK_%=\n" \
  "buffer_load_dword v99, off, s[40:43], s78 sc1\n" \
  "s_add_u32 s61, s61, 1\n" \
  "s_cmp_gt_u32 s61, 0x100000\n" \
  "s_cbranch_scc1 VABORT_%=\n" \
  "s_branch VWIT_%=\n" \
  "VWIT_OK_%=:\n" \
  "s_mov_b64 exec, -1\n" \
  "s_barrier\n" \
  ".rept %c[wb]\n" \
  "s_nop 7\n" \
  ".endr\n" \
  "s_branch VLOOP_%=\n" \
  "VMB_MISS_%=:\n" \
  "VMB_LOOP_%=:\n" \
  "buffer_load_dwordx4 v[112:115], v0, s[40:43], s49 offen sc1\n" \
  "buffer_load_dwordx4 v[116:119], v0, s[40:43], s49 offen offset:1024 sc1\n" \
  "s_waitcnt vmcnt(4)\n" \
  "v_max3_u32 v47, v96, v97, v98\n" \
  "v_max3_u32 v47, v47, v99, v100\n" \
  "v_max3_u32 v47, v47, v101, v102\n" \
  "v_max_u32 v47, v47, v103\n" \
  "v_cmp_eq_u32 vcc, -1, v47\n" \
  "s_cbranch_vccz VMB_GOT1_%=\n" \
  "buffer_load_dwordx4 v[96:99], v0, s[40:43], s49 offen sc1\n" \
  "buffer_load_dwordx4 v[100:103], v0, s[40:43], s49 offen offset:1024 sc1\n" \
  "s_waitcnt vmcnt(4)\n" \
  "v_max3_u32 v47, v104, v105, v106\n" \
  "v_max3_u32 v47, v47, v107, v108\n" \
  "v_max3_u32 v47, v47, v109, v110\n" \
  "v_max_u32 v47, v47, v111\n" \
  "v_cmp_eq_u32 vcc, -1, v47\n" \
  "s_cbranch_vccz VMB_GOT2_%=\n" \
  "buffer_load_dwordx4 v[104:107], v0, s[40:43], s49 offen sc1\n" \
  "buffer_load_dwordx4 v[108:111], v0, s[40:43], s49 offen offset:1024 sc1\n" \
  "s_waitcnt vmcnt(4)\n" \
  "v_max3_u32 v47, v112, v113, v114\n" \
  "v_max3_u32 v47, v47, v115, v116\n" \
  "v_max3_u32 v47, v47, v117, v118\n" \
  "v_max_u32 v47, v47, v119\n" \
  "v_cmp_eq_u32 vcc, -1, v47\n" \
  "s_cbranch_vccz VMB_GOT3_%=\n" \
  "s_add_u32 s61, s61, 1\n" \
  "s_cmp_gt_u32 s61, 0x40000\n" \
  "s_cbranch_scc1 VABORT_%=\n" \
  "s_branch VMB_LOOP_%=\n" \
  "VMB_GOT1_%=:\n" \
  "v_mov_b64 v[16:17], v[96:97]\n" \
  "v_mov_b64 v[18:19], v[98:99]\n" \
  "v_mov_b64 v[20:21], v[100:101]\n" \
  "v_mov_b64 v[22:23], v[102:103]\n" \
  "s_nop 1\n" \
  "s_branch VMB_OK_%=\n" \
  "VMB_GOT2_%=:\n" \
  "v_mov_b64 v[16:17], v[104:105]\n" \
  "v_mov_b64 v[18:19], v[106:107]\n" \
  "v_mov_b64 v[20:21], v[108:109]\n" \
  "v_mov_b64 v[22:23], v[110:111]\n" \
  "s_nop 1\n" \
  "s_branch VMB_OK_%=\n" \
  "VMB_GOT3_%=:\n" \
  "v_mov_b64 v[16:17], v[112:113]\n" \
  "v_mov_b64 v[18:19], v[114:115]\n" \
  "v_mov_b64 v[20:21], v[116:117]\n" \
  "v_mov_b64 v[22:23], v[118:119]\n" \
  "s_nop 1\n" \
  "s_branch VMB_OK_%=\n" \
  "VABORT_%=:\n" \
  "s_mov_b32 s59, 0x400000\n" \
  "s_mov_b64 exec, 1\n" \
  "buffer_store_dword v11, off, s[40:43], s59 sc1\n" \
  "s_mov_b64 exec, -1\n" \
  "VEND_%=:\n" \
  "s_mov_b64 exec, -1\n" \
  "s_waitcnt vmcnt(0) lgkmcnt(0)\n" \
  ""
#define VHEAD_ASM_CLOB "v0", "v1", "v2", "v3", "v4", "v5", "v6", "v7", "v8", "v9", "v10", "v11", "v12", "v13", "v14", "v15", "v16", "v17", "v18", "v19", "v20", "v21", "v22", "v23", "v24", "v25", "v26", "v27", "v28", "v29", "v30", "v31", "v32", "v33", "v34", "v35", "v36", "v37", "v38", "v39", "v40", "v41", "v42", "v43", "v44", "v45", "v46", "v47", "v48", "v49", "v50", "v51", "v52", "v53", "v54", "v55", "v56", "v57", "v58", "v59", "v60", "v61", "v62", "v63", "v64", "v65", "v66", "v67", "v68", "v69", "v70", "v71", "v72", "v73", "v74", "v75", "v76", "v77", "v78", "v79", "v80", "v81", "v82", "v83", "v84", "v85", "v86", "v87", "v88", "v89", "v90", "v91", "v92", "v93", "v94", "v95", "v96", "v97", "v98", "v99", "v100", "v101", "v102", "v103", "v104", "v105", "v106", "v107", "v108", "v109", "v110", "v111", "v112", "v113", "v114", "v115", "v116", "v117", "v118", "v119", "v120", "v121", "v122", "v123", "v124", "v125", "v126", "v127", "v128", "v129", "v130", "v131", "v132", "v133", "v134", "v135", "v136", "v137", "v138", "v139", "v140", "v141", "v142", "v143", "v144", "v145", "v146", "v147", "v148", "v149", "v150", "v151", "v152", "v153", "v154", "v155", "v156", "v157", "v158", "v159", "v160", "v161", "v162", "v163", "v164", "v165", "v166", "v167", "v168", "v169", "v170", "v171", "v172", "v173", "v174", "v175", "v176", "v177", "v178", "v179", "v180", "v181", "v182", "v183", "v184", "v185", "v186", "v187", "v188", "v189", "v190", "v191", "v192", "v193", "v194", "v195", "v196", "v197", "v198", "v199", "v200", "v201", "v202", "v203", "v204", "v205", "v206", "v207", "v208", "v209", "v210", "v211", "v212", "v213", "v214", "v215", "v216", "v217", "v218", "v219", "v220", "v221", "v222", "v223", "v224", "v225", "v226", "v227", "v228", "v229", "v230", "v231", "v232", "v233", "v234", "v235", "v236", "v237", "v238", "v239", "v240", "v241", "v242", "v243", "v244", "v245", "v246", "v247", "v248", "v249", "v250", "v251", "v252", "v253", "v254", "v255", "a0", "a1", "a2", "a3", "a4", "a5", "a6", "a7", "a8", "a9", "a10", "a11", "a12", "a13", "a14", "a15", "a16", "a17", "a18", "a19", "a20", "a21", "a22", "a23", "a24", "a25", "a26", "a27", "a28", "a29", "a30", "a31", "a32", "a33", "a34", "a35", "a36", "a37", "a38", "a39", "a40", "a41", "a42", "a43", "a44", "a45", "a46", "a47", "a48", "a49", "a50", "a51", "a52", "a53", "a54", "a55", "a56", "a57", "a58", "a59", "a60", "a61", "a62", "a63", "a64", "a65", "a66", "a67", "a68", "a69", "a70", "a71", "a72", "a73", "a74", "a75", "a76", "a77", "a78", "a79", "a80", "a81", "a82", "a83", "a84", "a85", "a86", "a87", "a88", "a89", "a90", "a91", "a92", "a93", "a94", "a95", "a96", "a97", "a98", "a99", "a100", "a101", "a102", "a103", "a104", "a105", "a106", "a107", "a108", "a109", "a110", "a111", "a112", "a113", "a114", "a115", "a116", "a117", "a118", "a119", "a120", "a121", "a122", "a123", "a124", "a125", "a126", "a127", "s40", "s41", "s42", "s43", "s44", "s45", "s46", "s47", "s48", "s49", "s50", "s51", "s52", "s53", "s54", "s55", "s56", "s57", "s58", "s59", "s60", "s61", "s62", "s63", "s64", "s65", "s66", "s67", "s68", "s69", "s70", "s71", "s72", "s73", "s74", "s75", "s76", "s77", "s78", "s79", "s80", "s81", "s82", "s83", "s84", "s85", "vcc", "scc", "memory"


template <bool HEAD, int WA, int WD, int WB, int WG>
__device__ __forceinline__ void chain_role(const Params& p, unsigned char* smem, const int h, const u32 sel0, const u32 sel1) {
    const int tid = threadIdx.x, lane = tid & 63, w = __builtin_amdgcn_readfirstlane(tid >> 6);
    const int c = lane & 15, g = lane >> 4;
    LAS u32* tab = (LAS u32*)((LAS unsigned char*)smem + L_TAB + w * 4096) + lane;
    tab[0 * 64] = lane * 16;
    tab[1 * 64] = ((lane >> 1) & 1) ? 0xEEEEu : 0x4444u;
    tab[2 * 64] = HEAD ? L_QK + g * 256 + (((16 * (w & 1) + c + 8 * g) & 31) * 8) + ((w >> 1) ? 0 : 4)
                       : L_QK + g * 256 + (((16 * (w >> 1) + c + 8 * g) & 31) * 8) + 2 * (w & 1) * 1024;
    tab[3 * 64] = L_QK + (c & 3) * 256 + (((4 * g + (c >> 2) + 8 * (c & 3)) & 31) * 8);
    tab[4 * 64] = L_QK + (c & 3) * 256 + (((16 + 4 * g + (c >> 2) + 8 * (c & 3)) & 31) * 8);
    tab[5 * 64] = (c & 2) ? L_O + (w >> 1) * 1024 + (g * 16 + 4 * (c >> 2) + 2 * (w & 1) + (c & 1)) * 16 : L_DUMP + lane * 16;
    tab[6 * 64] = L_VX + w * 2048 + lane * 16;
    tab[7 * 64] = (4 * (c >> 3) + g) * 128 + (c & 7) * 16;
    const int r = 4 * h + w;
    tab[8 * 64] = (4 * g + (c >> 2)) * 16384 + (c & 3) * 16 + (r >> 1) * 128 + (r & 1) * 64;
    tab[9 * 64] = c == 0 ? (u32)(((2 * (r >> 5) + ((r >> 4) & 1)) * 4 + 2 * ((r >> 3) & 1) + (r & 1)) * 256 + ((r >> 1) & 3) * 64 + g * 16) : 0x80000000u;
    tab[10 * 64] = ((h * DH + 8 * g) * DM + 128 * w + 8 * c) * 2;
    tab[11 * 64] = ((32 * (g >> 1) + 16 * (g & 1)) * HD + h * DH + 64 * (w & 1) + 4 * c) * 2;
    tab[12 * 64] = ((32 * (g >> 1) + 16 * (g & 1)) * HD + h * DH + 64 * (w >> 1) + 4 * c + 2 * (w & 1)) * 2;
    if (w == 0) {
        const int G = c >> 2, jsel = (c & 3) < 2 ? 2 * (2 * (G >> 1) + (c & 1)) + (G & 1) : -1;
        u32x4 sv;
#pragma unroll
        for (int k = 0; k < 4; ++k) sv[k] = (jsel == 2 * k ? 0x3F80u : 0u) | (jsel == 2 * k + 1 ? 0x3F800000u : 0u);
        *(LAS u32x4*)((LAS unsigned char*)smem + L_SEL + lane * 16) = sv;
    }
    asm volatile("s_waitcnt lgkmcnt(0)" ::: "memory");
    const unsigned long long ws = (unsigned long long)p.ws, wo = (unsigned long long)p.Wo, wv = (unsigned long long)p.Wv, wk = (unsigned long long)p.Wk;
    const unsigned long long wqk = (unsigned long long)((w >> 1) ? p.Wk : p.Wq);
    const u32 wslo = (u32)ws, wshi = (u32)(ws >> 32) & 0xFFFFu;
    const u32 wit = W_WIT + sel1 * 8192 + h * 128;
    const u32 vf0 = W_VMF + (h >> 1) * 16384 + sel1 * 8192 + (h & 1) * 4096;
#define CHAIN_IN [wslo] "s"(wslo), [wshi] "s"(wshi), [wssz] "s"((u32)W_END), [mem0] "s"(W_MEM + sel0 * 8192), [h] "s"(h), [w] "s"(w), [wit] "s"(wit), [vf0] "s"(vf0), [wb] "i"(WB), [wg] "i"(WG)
#define HEAD_IN CHAIN_IN, [ring0] "s"(W_RING0 + sel0 * 8192), [ring1] "s"(W_RING1 + sel1 * 8192), [wolo] "s"((u32)wo), [wohi] "s"((u32)(wo >> 32) & 0xFFFFu), \
        [wqklo] "s"((u32)wqk), [wqkhi] "s"((u32)(wqk >> 32) & 0xFFFFu), [wa] "i"(WA), [wd] "i"(WD)
#define VHEAD_IN CHAIN_IN, [wvlo] "s"((u32)wv), [wvhi] "s"((u32)(wv >> 32) & 0xFFFFu), [wklo] "s"((u32)wk), [wkhi] "s"((u32)(wk >> 32) & 0xFFFFu)
    if constexpr (HEAD) {
        asm volatile(HEAD_ASM : : HEAD_IN : HEAD_ASM_CLOB);
    } else {
        asm volatile(VHEAD_ASM : : VHEAD_IN : VHEAD_ASM_CLOB);
    }
}
typedef __attribute__((address_space(1))) const u32x4 gc_u32x4;
__device__ __forceinline__ f32x4 mfma16b(u32x4 a, u32x4 b, f32x4 c) {
    return __builtin_amdgcn_mfma_f32_16x16x32_bf16(__builtin_bit_cast(bf16x8, a), __builtin_bit_cast(bf16x8, b), c, 0, 0, 0);
}
__device__ __forceinline__ u32 perm_lo(u32 r0, u32 r1) { return __builtin_amdgcn_perm(r1, r0, 0x05040100u); }
__device__ __forceinline__ u32 perm_hi(u32 r0, u32 r1) { return __builtin_amdgcn_perm(r1, r0, 0x07060302u); }
#define SCHED_FENCE() __builtin_amdgcn_sched_barrier(0)
#define DRAIN() asm volatile("s_waitcnt vmcnt(0)" ::: "memory")
constexpr u32 SPIN_CAP = 1u << 20;

#define MF_AW(acc, w, x) asm volatile("v_mfma_f32_16x16x32_bf16 %0, %1, %2, %0" : "+v"(acc) : "a"(w), "v"(x))
#define MF_BW(acc, x, w) asm volatile("v_mfma_f32_16x16x32_bf16 %0, %1, %2, %0" : "+v"(acc) : "v"(x), "a"(w))
#define ACC_READY4(a, b, c, d) asm volatile("s_nop 1" : "+v"(a), "+v"(b), "+v"(c), "+v"(d))
#define ACC_READY2(a, b) asm volatile("s_nop 1" : "+v"(a), "+v"(b))
#define MFMA_DONE4(a, b, c, d) asm volatile("s_nop 7" : "+v"(a), "+v"(b), "+v"(c), "+v"(d))
#define MFMA_DONE2(a, b) asm volatile("s_nop 7" : "+v"(a), "+v"(b))

__device__ __forceinline__ bool wait_word(gu32* word, u32 want, gu32* ctrl) {
    for (u32 spins = 0;; ++spins) {
        if ((u32)__builtin_amdgcn_readfirstlane(__hip_atomic_load(word, RLX_AGENT)) == want) return true;
        if (spins > SPIN_CAP) { __hip_atomic_store(ctrl, 1u, RLX_AGENT); return false; }
        if ((spins & 63u) == 63u && __builtin_amdgcn_readfirstlane(__hip_atomic_load(ctrl, RLX_AGENT)) != 0) return false;
        __builtin_amdgcn_s_sleep(8);
    }
}

__device__ __forceinline__ bool wait_quad(__amdgpu_buffer_rsrc_t rs, u32 off, u32 w0, u32 w1, u32 w2, u32 w3, u32 mask, gu32* ctrl, int nap) {
    for (u32 spins = 0;; ++spins) {
        asm volatile("" ::: "memory");
        const u32x4 f = __builtin_amdgcn_raw_buffer_load_b128(rs, 0, off, AUX_SC1);
        const u32 bad = ((f.x ^ w0) & -(mask & 1u)) | ((f.y ^ w1) & -((mask >> 1) & 1u)) | ((f.z ^ w2) & -((mask >> 2) & 1u)) | ((f.w ^ w3) & -((mask >> 3) & 1u));
        if (__builtin_amdgcn_readfirstlane(bad) == 0u) return true;
        if (spins > SPIN_CAP) { __hip_atomic_store(ctrl, 1u, RLX_AGENT); return false; }
        if ((spins & 63u) == 63u && __builtin_amdgcn_readfirstlane(__hip_atomic_load(ctrl, RLX_AGENT)) != 0) return false;
        if (nap) __builtin_amdgcn_s_sleep(4);
    }
}

template <bool VT>
__device__ __forceinline__ void proj_role(const Params& p, const int type, const int h) {
    const int tid = threadIdx.x, lane = tid & 63, w = __builtin_amdgcn_readfirstlane(tid >> 6);
    const int c = lane & 15, g = lane >> 4, half = w & 1, par = w >> 1;
    const __amdgpu_buffer_rsrc_t rs = __builtin_amdgcn_make_buffer_rsrc(p.ws, 0, (int)W_END, RSRC_W3);
    gu32* flags = (gu32*)(p.ws + W_FLAG);
    const u16* Wsrc = type == 0 ? p.Wk : type == 1 ? p.Wv : p.Wq;
    const __amdgpu_buffer_rsrc_t rw = __builtin_amdgcn_make_buffer_rsrc((void*)Wsrc, 0, DM * HD * 2, RSRC_W3);
    u32x4 W[4][16];
    {
        const int vo = (8 * g * HD + h * DH + 64 * half + 4 * c) * 2;
        constexpr int LAG = 6;
        u32x2 raw[LAG + 1][8];
#pragma unroll
        for (int q = 0; q < 16 + LAG; ++q) {
            if (q < 16) {
#pragma unroll
                for (int j = 0; j < 8; ++j) raw[q % (LAG + 1)][j] = __builtin_amdgcn_raw_buffer_load_b64(rw, vo, (32 * q + j) * HD * 2, 0);
            }
            SCHED_FENCE();
            if (q >= LAG) {
                const int kk = q - LAG;
#pragma unroll
                for (int jj = 0; jj < 4; ++jj) {
                    const u32x2 r0 = raw[kk % (LAG + 1)][2 * jj], r1 = raw[kk % (LAG + 1)][2 * jj + 1];
                    W[0][kk][jj] = perm_lo(r0.x, r1.x); W[1][kk][jj] = perm_hi(r0.x, r1.x);
                    W[2][kk][jj] = perm_lo(r0.y, r1.y); W[3][kk][jj] = perm_hi(r0.y, r1.y);
                }
                asm volatile("" : "+a"(W[0][kk]), "+a"(W[1][kk]), "+a"(W[2][kk]), "+a"(W[3][kk]));
            }
            SCHED_FENCE();
        }
    }
    const u32 obase = type == 0 ? W_KX : type == 1 ? W_VX : W_QX;
    gu32* cnt0 = flags + F_SH + h * 16 + (type == 2 ? 1 : 0);
    const u32 xoff = W_XF + lane * 16;
    u32x4 X[32];
#pragma unroll
    for (int i = 0; i < 32; ++i) X[i] = __builtin_amdgcn_raw_buffer_load_b128(rs, xoff, (u32)(par * 32 + i) * 1024, 0);
    for (int s = par; s < NSEG; s += 2) {
        f32x4 acc[2][4];
#pragma unroll
        for (int tk = 0; tk < 2; ++tk)
#pragma unroll
            for (int t = 0; t < 4; ++t) acc[tk][t] = f32x4{0.f, 0.f, 0.f, 0.f};
        const u32 xn = (u32)((s + 2) * 32) * 1024;
        if constexpr (VT) {
            ACC_READY4(acc[0][0], acc[0][1], acc[0][2], acc[0][3]); ACC_READY4(acc[1][0], acc[1][1], acc[1][2], acc[1][3]);
#pragma unroll
            for (int i = 0; i < 32; ++i) {
#pragma unroll
                for (int t = 0; t < 4; ++t) MF_BW(acc[i >> 4][t], X[i], W[t][i & 15]);
                X[i] = __builtin_amdgcn_raw_buffer_load_b128(rs, xoff, xn + i * 1024, 0);
                SCHED_FENCE();
            }
            MFMA_DONE4(acc[0][0], acc[0][1], acc[0][2], acc[0][3]); MFMA_DONE4(acc[1][0], acc[1][1], acc[1][2], acc[1][3]);
        } else {
            ACC_READY4(acc[0][0], acc[0][1], acc[0][2], acc[0][3]); ACC_READY4(acc[1][0], acc[1][1], acc[1][2], acc[1][3]);
#pragma unroll
            for (int i = 0; i < 32; ++i) {
#pragma unroll
                for (int t = 0; t < 4; ++t) MF_AW(acc[i >> 4][t], W[t][i & 15], X[i]);
                X[i] = __builtin_amdgcn_raw_buffer_load_b128(rs, xoff, xn + i * 1024, 0);
                SCHED_FENCE();
            }
            MFMA_DONE4(acc[0][0], acc[0][1], acc[0][2], acc[0][3]); MFMA_DONE4(acc[1][0], acc[1][1], acc[1][2], acc[1][3]);
        }
        if (s >= 6) { asm volatile("s_waitcnt vmcnt(32)" ::: "memory"); if (lane == 0) __hip_atomic_fetch_add(cnt0 + (s - 2) * NH * 16, 1u, RLX_AGENT); }
        const u32 ob = obase + (u32)(s * NH + h) * 8192;
        if constexpr (VT) {
#pragma unroll
            for (int t = 0; t < 4; ++t) {
                const u32x4 v = {cvtpk(acc[0][t][0], acc[0][t][1]), cvtpk(acc[0][t][2], acc[0][t][3]), cvtpk(acc[1][t][0], acc[1][t][1]), cvtpk(acc[1][t][2], acc[1][t][3])};
                __builtin_amdgcn_raw_buffer_store_b128(v, rs, lane * 16, ob + (half * 4 + t) * 1024, AUX_SC1);
            }
        } else {
#pragma unroll
            for (int tk = 0; tk < 2; ++tk)
#pragma unroll
                for (int pp = 0; pp < 2; ++pp) {
                    const u32x4 v = {cvtpk(acc[tk][2 * pp][0], acc[tk][2 * pp][1]), cvtpk(acc[tk][2 * pp][2], acc[tk][2 * pp][3]),
                                     cvtpk(acc[tk][2 * pp + 1][0], acc[tk][2 * pp + 1][1]), cvtpk(acc[tk][2 * pp + 1][2], acc[tk][2 * pp + 1][3])};
                    __builtin_amdgcn_raw_buffer_store_b128(v, rs, lane * 16, ob + (tk * 4 + half * 2 + pp) * 1024, AUX_SC1);
                }
        }
        if (s < 4 || s + 2 >= NSEG) { DRAIN(); if (lane == 0) __hip_atomic_fetch_add(cnt0 + s * NH * 16, 1u, RLX_AGENT); }
    }
}

__device__ __forceinline__ float max3f(float a, float b, float c) { float r; asm("v_max3_f32 %0, %1, %2, %3" : "=v"(r) : "v"(a), "v"(b), "v"(c)); return r; }
__device__ __forceinline__ float xrow_max(float x) {
    auto s = __builtin_amdgcn_permlane16_swap(__float_as_uint(x), __float_as_uint(x), false, false);
    x = fmaxf(__uint_as_float(s[0]), __uint_as_float(s[1]));
    auto t = __builtin_amdgcn_permlane32_swap(__float_as_uint(x), __float_as_uint(x), false, false);
    return fmaxf(__uint_as_float(t[0]), __uint_as_float(t[1]));
}
__device__ __forceinline__ float xrow_sum(float x) {
    auto s = __builtin_amdgcn_permlane16_swap(__float_as_uint(x), __float_as_uint(x), false, false);
    x = __uint_as_float(s[0]) + __uint_as_float(s[1]);
    auto t = __builtin_amdgcn_permlane32_swap(__float_as_uint(x), __float_as_uint(x), false, false);
    return __uint_as_float(t[0]) + __uint_as_float(t[1]);
}
__device__ __forceinline__ void xattn_role(const Params& p, const int b) {
    const int tid = threadIdx.x, lane = tid & 63, w = __builtin_amdgcn_readfirstlane(tid >> 6);
    const int c = lane & 15, g = lane >> 4;
    const int h = b, tq = w & 1, par = w >> 1;
    const __amdgpu_buffer_rsrc_t rs = __builtin_amdgcn_make_buffer_rsrc(p.ws, 0, (int)W_END, RSRC_W3);
    gu32* flags = (gu32*)(p.ws + W_FLAG);
    constexpr float SC = 0.08838834764831845f * 1.4426950408889634f;
    bool known = false;
    int pend = -1;
    for (int s = par; s < NSEG; s += 2) {
        const u32 flo = W_FLAG + (F_SH + (s * NH + h) * 16) * 4;
        if (!known && !wait_quad(rs, flo, 4u, 2u, 0u, 0u, 3u, flags, 1)) return;
        const u32 sh = (u32)(s * NH + h);
        u32x4 Q[4], KX[2][4], VX[8];
#pragma unroll
        for (int ks = 0; ks < 4; ++ks) Q[ks] = __builtin_amdgcn_raw_buffer_load_b128(rs, lane * 16, W_QX + sh * 8192 + (tq * 4 + ks) * 1024, AUX_SC1);
#pragma unroll
        for (int i = 0; i < 8; ++i) KX[i >> 2][i & 3] = __builtin_amdgcn_raw_buffer_load_b128(rs, lane * 16, W_KX + sh * 8192 + i * 1024, AUX_SC1);
        f32x4 T1 = {0.f, 0.f, 0.f, 0.f}, T2 = T1, T0 = T1;
        u32x4 KM[4]; u32x2 VM[4];
        if (!known) {
#pragma unroll
            for (int i = 0; i < 8; ++i) VX[i] = __builtin_amdgcn_raw_buffer_load_b128(rs, lane * 16, W_VX + sh * 8192 + i * 1024, AUX_SC1);
#pragma unroll
            for (int ks = 0; ks < 4; ++ks) { T1 = mfma16b(KX[0][ks], Q[ks], T1); T2 = mfma16b(KX[1][ks], Q[ks], T2); }
            asm volatile("" : "+v"(T1), "+v"(T2));
            if (!wait_quad(rs, flo + 32, 0xFFFFFFFFu, 0xFFFFFFFFu, 0xFFFFFFFFu, 0xFFFFFFFFu, 15u, flags, 0)) return;
#pragma unroll
            for (int ks = 0; ks < 4; ++ks) KM[ks] = __builtin_amdgcn_raw_buffer_load_b128(rs, lane * 16, W_KM + sh * 4096 + ks * 1024, AUX_SC1);
#pragma unroll
            for (int k = 0; k < 4; ++k) VM[k] = __builtin_amdgcn_raw_buffer_load_b64(rs, lane * 8, W_VM + sh * 2048 + k * 512, AUX_SC1);
        } else {
#pragma unroll
            for (int ks = 0; ks < 4; ++ks) KM[ks] = __builtin_amdgcn_raw_buffer_load_b128(rs, lane * 16, W_KM + sh * 4096 + ks * 1024, AUX_SC1);
#pragma unroll
            for (int i = 0; i < 8; ++i) VX[i] = __builtin_amdgcn_raw_buffer_load_b128(rs, lane * 16, W_VX + sh * 8192 + i * 1024, AUX_SC1);
#pragma unroll
            for (int k = 0; k < 4; ++k) VM[k] = __builtin_amdgcn_raw_buffer_load_b64(rs, lane * 8, W_VM + sh * 2048 + k * 512, AUX_SC1);
#pragma unroll
            for (int ks = 0; ks < 4; ++ks) { T1 = mfma16b(KX[0][ks], Q[ks], T1); T2 = mfma16b(KX[1][ks], Q[ks], T2); }
        }
        const u32 fln = flo + 2 * NH * 64;
        const u32x4 fa = __builtin_amdgcn_raw_buffer_load_b128(rs, 0, fln, AUX_SC1), fb = __builtin_amdgcn_raw_buffer_load_b128(rs, 0, fln + 32, AUX_SC1);
#pragma unroll
        for (int ks = 0; ks < 4; ++ks) T0 = mfma16b(KM[ks], Q[ks], T0);
        const int qx = 16 * tq + c;
        const float NEG = -1e30f;
#pragma unroll
        for (int r = 0; r < 4; ++r) { if (4 * g + r > qx) T1[r] = NEG; if (16 + 4 * g + r > qx) T2[r] = NEG; }
        float mx = max3f(max3f(T0[0], T0[1], T1[0]), max3f(T1[1], T1[2], T1[3]), max3f(T2[0], T2[1], fmaxf(T2[2], T2[3])));
        mx = xrow_max(mx);
        const float nb = -mx * SC;
#pragma unroll
        for (int r = 0; r < 4; ++r) { T1[r] = __builtin_amdgcn_exp2f(fmaf(T1[r], SC, nb)); T2[r] = __builtin_amdgcn_exp2f(fmaf(T2[r], SC, nb)); }
        T0[0] = __builtin_amdgcn_exp2f(fmaf(T0[0], SC, nb)); T0[1] = __builtin_amdgcn_exp2f(fmaf(T0[1], SC, nb));
        const float l = xrow_sum(((T1[0] + T1[1]) + (T1[2] + T1[3])) + ((T2[0] + T2[1]) + (T2[2] + T2[3])) + (T0[0] + T0[1]));
        const float rl = __builtin_amdgcn_rcpf(l);
        const u32x4 PX = {cvtpk(T1[0], T1[1]), cvtpk(T1[2], T1[3]), cvtpk(T2[0], T2[1]), cvtpk(T2[2], T2[3])};
        const u32x4 PM = {cvtpk(T0[0], T0[1]), 0u, 0u, 0u};
        f32x4 o[8];
#pragma unroll
        for (int i = 0; i < 8; ++i) {
            const u32 vm = VM[2 * (i >> 2) + ((i & 3) >> 1)][i & 1];
            o[i] = mfma16b(VX[i], PX, f32x4{0.f, 0.f, 0.f, 0.f});
            o[i] = mfma16b(u32x4{vm, 0u, 0u, 0u}, PM, o[i]);
        }
        asm volatile("" :: "v"(o[0]), "v"(o[7]) : "memory");
        if (pend >= 0 && lane == 0) __hip_atomic_fetch_add(flags + F_TT + pend * 16, 1u, RLX_AGENT);
        const u32 ob = W_OX + (u32)((2 * s + tq) * NH + h) * 4096;
#pragma unroll
        for (int ks = 0; ks < 4; ++ks) {
            const f32x4 a = o[(ks >> 1) * 4 + 2 * (ks & 1)], b2 = o[(ks >> 1) * 4 + 2 * (ks & 1) + 1];
            const u32x4 v = {cvtpk(a[0] * rl, a[1] * rl), cvtpk(a[2] * rl, a[3] * rl), cvtpk(b2[0] * rl, b2[1] * rl), cvtpk(b2[2] * rl, b2[3] * rl)};
            __builtin_amdgcn_raw_buffer_store_b128(v, rs, lane * 16, ob + ks * 1024, AUX_SC1);
        }
        known = __builtin_amdgcn_readfirstlane((fa.x ^ 4u) | (fa.y ^ 2u) | ~(fb.x & fb.y & fb.z & fb.w)) == 0u && s + 2 < NSEG;
        if (known) pend = 2 * s + tq;
        else { pend = -1; DRAIN(); if (lane == 0) __hip_atomic_fetch_add(flags + F_TT + (2 * s + tq) * 16, 1u, RLX_AGENT); }
    }
}

constexpr int C_REP = 6;
__device__ __forceinline__ void oproj_role(const Params& p, unsigned char* smem, const int ci) {
    const int tid = threadIdx.x, lane = tid & 63, w = __builtin_amdgcn_readfirstlane(tid >> 6);
    const int c = lane & 15, g = lane >> 4;
    const int ct = ci & 15, rep = ci >> 4;
    if (rep >= C_REP) return;
    const __amdgpu_buffer_rsrc_t rs = __builtin_amdgcn_make_buffer_rsrc(p.ws, 0, (int)W_END, RSRC_W3);
    const __amdgpu_buffer_rsrc_t ro = __builtin_amdgcn_make_buffer_rsrc((void*)p.Wo, 0, HD * DM * 2, RSRC_W3);
    gu32* flags = (gu32*)(p.ws + W_FLAG);
    u32x4 W[2][32];
    {
        const int vo = ((8 * w * DH + 16 * g) * DM + 32 * ct + 2 * c) * 2;
        constexpr int LAG = 6;
        u32 raw[LAG + 1][8];
#pragma unroll
        for (int q = 0; q < 32 + LAG; ++q) {
            if (q < 32) {
                const int hh = q >> 2, half = (q >> 1) & 1, pp = q & 1;
#pragma unroll
                for (int j = 0; j < 8; ++j) raw[q % (LAG + 1)][j] = __builtin_amdgcn_raw_buffer_load_b32(ro, vo, (hh * DH + 64 * half + 4 * (j & 3) + 2 * pp + (j >> 2)) * DM * 2, 0);
            }
            SCHED_FENCE();
            if (q >= LAG) {
                const int k = q - LAG;
#pragma unroll
                for (int jj = 0; jj < 4; ++jj) {
                    const u32 r0 = raw[k % (LAG + 1)][2 * jj], r1 = raw[k % (LAG + 1)][2 * jj + 1];
                    W[0][k][jj] = perm_lo(r0, r1); W[1][k][jj] = perm_hi(r0, r1);
                }
                asm volatile("" : "+a"(W[0][k]), "+a"(W[1][k]));
            }
            SCHED_FENCE();
        }
    }
    if (!wait_word(flags + F_MOE, 14u, flags)) return;
    const int idx0 = (int)__builtin_amdgcn_readfirstlane(__hip_atomic_load(flags + F_MOE + 16, RLX_AGENT));
    const int idx1 = (int)__builtin_amdgcn_readfirstlane(__hip_atomic_load(flags + F_MOE + 17, RLX_AGENT));
    LAS f32x4* red = (LAS f32x4*)smem;
    int it = 0;
    for (int tt = rep; tt < 2 * NSEG; tt += C_REP, ++it) {
        if (!wait_quad(rs, W_FLAG + (F_TT + tt * 16) * 4, 32u, 0u, 0u, 0u, 1u, flags, 1)) return;
        f32x4 a0 = {0.f, 0.f, 0.f, 0.f}, a1 = a0;
        const u32 ob = W_OX + (u32)(tt * NH + 8 * w) * 4096;
        u32x4 O[32];
#pragma unroll
        for (int k = 0; k < 32; ++k) O[k] = __builtin_amdgcn_raw_buffer_load_b128(rs, lane * 16, ob + k * 1024, AUX_SC1);
        const int row = 16 * tt + c, col = 32 * ct + 8 * g;
        u32x4 xv = {0, 0, 0, 0}, xnv = xv;
        if (w == 0) { xv = *(const u32x4*)(p.x + row * DM + col); xnv = __builtin_amdgcn_raw_buffer_load_b128(rs, (row * DM + col) * 2, W_XN, 0); }
#pragma unroll
        for (int k = 0; k < 32; ++k) { if (k == 0) ACC_READY2(a0, a1); MF_AW(a0, W[0][k], O[k]); MF_AW(a1, W[1][k], O[k]); }
        MFMA_DONE2(a0, a1);
        const int buf = (it & 1) * 512;
        red[buf + (w * 2 + 0) * 64 + lane] = a0; red[buf + (w * 2 + 1) * 64 + lane] = a1;
        __syncthreads();
        if (w == 0) {
            f32x4 s0 = red[buf + lane], s1 = red[buf + 64 + lane];
#pragma unroll
            for (int ww = 1; ww < 4; ++ww) { s0 += red[buf + (ww * 2) * 64 + lane]; s1 += red[buf + (ww * 2 + 1) * 64 + lane]; }
            float upd[8];
#pragma unroll
            for (int j = 0; j < 8; ++j) upd[j] = 0.f;
            if (row == idx0 || row == idx1) {
                const int k = row == idx0 ? 0 : 1;
                const float rv = __builtin_bit_cast(float, __builtin_amdgcn_raw_buffer_load_b32(rs, row * 4, W_R, 0));
                const float rsel = rbf(1.f / (1.f + __expf(-rv)));
#pragma unroll
                for (int j = 0; j < 8; ++j) {
                    float y = 0.f;
                    for (int m = 0; m < 14; ++m) y += __builtin_bit_cast(float, __builtin_amdgcn_raw_buffer_load_b32(rs, ((m * 2 + k) * DM + col + j) * 4, W_MOE, AUX_SC1));
                    upd[j] = rbf(rbf(y) * rsel);
                }
            }
            u32 o[4];
#pragma unroll
            for (int r = 0; r < 4; ++r) {
                const float A0 = rbf(s0[r]), A1 = rbf(s1[r]);
                const float x0 = __uint_as_float(xv[r] << 16), x1 = __uint_as_float(xv[r] & 0xFFFF0000u);
                const float n0 = __uint_as_float(xnv[r] << 16), n1 = __uint_as_float(xnv[r] & 0xFFFF0000u);
                o[r] = cvtpk(rbf(x0 + A0) + rbf(n0 + upd[2 * r]), rbf(x1 + A1) + rbf(n1 + upd[2 * r + 1]));
            }
            *(u32x4*)(p.out + row * DM + col) = u32x4{o[0], o[1], o[2], o[3]};
        }
    }
}

__device__ __forceinline__ void moe_role(const Params& p, unsigned char* smem, const int m) {
    const int tid = threadIdx.x, lane = tid & 63, w = tid >> 6;
    const __amdgpu_buffer_rsrc_t rs = __builtin_amdgcn_make_buffer_rsrc(p.ws, 0, (int)W_END, RSRC_W3);
    gu32* flags = (gu32*)(p.ws + W_FLAG);
    const float* r = (const float*)(p.ws + W_R);
    typedef unsigned long long u64;
    u64 k0 = 0, k1 = 0;
    for (int i = tid; i < S_TOK; i += 256) {
        const u32 b = __float_as_uint(r[i]);
        const u64 k = ((u64)((b & 0x80000000u) ? ~b : (b | 0x80000000u)) << 32) | (u32)(0x7FFFFFFF - i);
        const u64 lo = k < k0 ? k : k0; k0 = k < k0 ? k0 : k; k1 = k1 < lo ? lo : k1;
    }
    for (int d = 32; d; d >>= 1) {
        const u64 a0 = __shfl_xor(k0, d), a1 = __shfl_xor(k1, d);
        const u64 lo = a0 < k0 ? a0 : k0; k0 = a0 < k0 ? k0 : a0;
        const u64 m1 = a1 < k1 ? k1 : a1; k1 = m1 < lo ? lo : m1;
    }
    LAS u64* sk = (LAS u64*)smem;
    if (lane == 0) { sk[2 * w] = k0; sk[2 * w + 1] = k1; }
    __syncthreads();
    k0 = sk[0]; k1 = sk[1];
#pragma unroll
    for (int q = 1; q < 4; ++q) {
        const u64 a0 = sk[2 * q], a1 = sk[2 * q + 1];
        const u64 lo = a0 < k0 ? a0 : k0; k0 = a0 < k0 ? k0 : a0;
        const u64 m1 = a1 < k1 ? k1 : a1; k1 = m1 < lo ? lo : m1;
    }
    const int i0 = 0x7FFFFFFF - (int)(u32)k0, i1 = 0x7FFFFFFF - (int)(u32)k1;
    __syncthreads();
    LAS float* xs = (LAS float*)(smem + 8192);
    const u16* xn = (const u16*)(p.ws + W_XN);
    for (int i = tid; i < 1024; i += 256) xs[i] = bf2f(xn[(i < 512 ? i0 : i1) * DM + (i & 511)]);
    __syncthreads();
    const int kq = tid >> 4, cq = tid & 15, f0 = 128 * m + 8 * cq;
    float ga[2][8], ua[2][8];
#pragma unroll
    for (int k = 0; k < 2; ++k)
#pragma unroll
        for (int j = 0; j < 8; ++j) { ga[k][j] = 0.f; ua[k][j] = 0.f; }
#pragma unroll 8
    for (int i = 0; i < 32; ++i) {
        const int d = kq + 16 * i;
        const u32x4 wg = *(const u32x4*)(p.wg + d * FF + f0), wu = *(const u32x4*)(p.wu + d * FF + f0);
        const float x0 = xs[d], x1 = xs[512 + d];
#pragma unroll
        for (int j = 0; j < 4; ++j) {
            const float g0 = __uint_as_float(wg[j] << 16), g1 = __uint_as_float(wg[j] & 0xFFFF0000u), u0 = __uint_as_float(wu[j] << 16), u1 = __uint_as_float(wu[j] & 0xFFFF0000u);
            ga[0][2 * j] += x0 * g0; ga[0][2 * j + 1] += x0 * g1; ga[1][2 * j] += x1 * g0; ga[1][2 * j + 1] += x1 * g1;
            ua[0][2 * j] += x0 * u0; ua[0][2 * j + 1] += x0 * u1; ua[1][2 * j] += x1 * u0; ua[1][2 * j + 1] += x1 * u1;
        }
    }
    LAS float* part = (LAS float*)(smem + 16384);
#pragma unroll
    for (int k = 0; k < 2; ++k)
#pragma unroll
        for (int j = 0; j < 8; ++j) { part[((kq * 2 + 0) * 2 + k) * 128 + 8 * cq + j] = ga[k][j]; part[((kq * 2 + 1) * 2 + k) * 128 + 8 * cq + j] = ua[k][j]; }
    __syncthreads();
    LAS float* hh = (LAS float*)(smem + 12288);
    {
        const int k = tid >> 7, f = tid & 127;
        float gs = 0.f, us = 0.f;
        for (int q = 0; q < 16; ++q) { gs += part[((q * 2 + 0) * 2 + k) * 128 + f]; us += part[((q * 2 + 1) * 2 + k) * 128 + f]; }
        gs = rbf(gs); us = rbf(us);
        const float sg = rbf(gs / (1.f + __expf(-gs)));
        hh[k * 128 + f] = rbf(sg * us);
    }
    __syncthreads();
    {
        const int k4 = tid >> 6, c8 = 8 * (tid & 63);
        float ya[2][8];
#pragma unroll
        for (int k = 0; k < 2; ++k)
#pragma unroll
            for (int j = 0; j < 8; ++j) ya[k][j] = 0.f;
#pragma unroll 8
        for (int i = 0; i < 32; ++i) {
            const int f = 32 * k4 + i;
            const u32x4 wd = *(const u32x4*)(p.wd + (128 * m + f) * DM + c8);
            const float h0 = hh[f], h1 = hh[128 + f];
#pragma unroll
            for (int j = 0; j < 4; ++j) {
                const float d0 = __uint_as_float(wd[j] << 16), d1 = __uint_as_float(wd[j] & 0xFFFF0000u);
                ya[0][2 * j] += h0 * d0; ya[0][2 * j + 1] += h0 * d1; ya[1][2 * j] += h1 * d0; ya[1][2 * j + 1] += h1 * d1;
            }
        }
        __syncthreads();
#pragma unroll
        for (int k = 0; k < 2; ++k)
#pragma unroll
            for (int j = 0; j < 8; ++j) part[(k4 * 2 + k) * 512 + c8 + j] = ya[k][j];
    }
    __syncthreads();
    for (int i = tid; i < 1024; i += 256) {
        const float y = (part[i] + part[1024 + i]) + (part[2048 + i] + part[3072 + i]);
        __builtin_amdgcn_raw_buffer_store_b32(__float_as_uint(y), rs, (m * 1024 + i) * 4, W_MOE, AUX_SC1);
    }
    if (m == 0 && tid == 0) { __hip_atomic_store(flags + F_MOE + 16, (u32)i0, RLX_AGENT); __hip_atomic_store(flags + F_MOE + 17, (u32)i1, RLX_AGENT); }
    DRAIN();
    __syncthreads();
    if (tid == 0) __hip_atomic_fetch_add(flags + F_MOE, 1u, RLX_AGENT);
}
__global__ void __launch_bounds__(256) k_init(Params p) {
    const int tid = threadIdx.x, lane = tid & 63, w = tid >> 6, b = blockIdx.x;
    unsigned char* ws = p.ws;
    if (b < 8) {
        __shared__ u32 lat[4];
        const __amdgpu_buffer_rsrc_t rs = __builtin_amdgcn_make_buffer_rsrc(ws, 0, (int)W_END, RSRC_W3);
        u32 best = 0xFFFFFFFFu;
        const int off0 = ((w >> 1) ? 3 * MB + 640 * 1024 : 4 * MB + 512 * 1024) + (w & 1) * 8192 + b * 16384 + (lane & 7) * 16;
        const u32x4 v = {0u, 0u, 0u, 0u};
#pragma unroll
        for (int rep = 0; rep < 4; ++rep) __builtin_amdgcn_raw_buffer_store_b128(v, rs, off0 + rep * 1024, 0, AUX_SC1);
        asm volatile("s_waitcnt vmcnt(0)" ::: "memory");
        for (int rep = 0; rep < 4; ++rep) {
            const unsigned long long t0 = __builtin_amdgcn_s_memtime();
            asm volatile("s_waitcnt lgkmcnt(0)" ::: "memory");
            const u32x4 a = __builtin_amdgcn_raw_buffer_load_b128(rs, off0 + rep * 1024, 0, AUX_SC1);
            asm volatile("s_waitcnt vmcnt(0)" :: "v"(a) : "memory");
            const unsigned long long t1 = __builtin_amdgcn_s_memtime();
            best = min(best, (u32)(t1 - t0));
        }
        if (lane == 0) lat[w] = best;
        __syncthreads();
        if (tid < 4) ((u32*)(ws + W_SEL))[(xcc_id() & 7) * 4 + tid] = lat[tid];
    }
    {
        const int row = b * 4 + w;
        const u32x4 xv = *(const u32x4*)(p.x + row * DM + lane * 8);
        const u32x4 wv = *(const u32x4*)(p.wr + lane * 8);
        float v[8]; float s = 0.f;
#pragma unroll
        for (int j = 0; j < 4; ++j) { v[2 * j] = __uint_as_float(xv[j] << 16); v[2 * j + 1] = __uint_as_float(xv[j] & 0xFFFF0000u); s += v[2 * j] + v[2 * j + 1]; }
        for (int d = 32; d; d >>= 1) s += __shfl_xor(s, d);
        const float mean = s * (1.f / DM);
        float q = 0.f;
#pragma unroll
        for (int j = 0; j < 8; ++j) { const float d = v[j] - mean; q += d * d; }
        for (int d = 32; d; d >>= 1) q += __shfl_xor(q, d);
        const float mb = rbf(mean), vb = rbf(q * (1.f / DM));
        const float den = rbf(sqrtf(rbf(vb + 1e-5f)));
        float y[8], rr = 0.f;
#pragma unroll
        for (int j = 0; j < 8; ++j) {
            y[j] = rbf(rbf(v[j] - mb) / den);
            rr += y[j] * ((j & 1) ? __uint_as_float(wv[j >> 1] & 0xFFFF0000u) : __uint_as_float(wv[j >> 1] << 16));
        }
        for (int d = 32; d; d >>= 1) rr += __shfl_xor(rr, d);
        const u32x4 o = {cvtpk(y[0], y[1]), cvtpk(y[2], y[3]), cvtpk(y[4], y[5]), cvtpk(y[6], y[7])};
        *(u32x4*)(ws + W_XN + (row * DM + lane * 8) * 2) = o;
        *(u32x4*)(ws + W_XF + (((row >> 4) * 16 + (lane >> 2)) * 64 + (lane & 3) * 16 + (row & 15)) * 16) = o;
        if (lane == 0) ((float*)(ws + W_R))[row] = rbf(rr);
    }
    {
        const u32x4 ones = {0xFFFFFFFFu, 0xFFFFFFFFu, 0xFFFFFFFFu, 0xFFFFFFFFu}, zero = {0u, 0u, 0u, 0u};
        const int gt = b * 256 + tid, nt = gridDim.x * 256;
        for (u32 i = 1024 + gt; i < W_NEAR_END / 16; i += nt) *(u32x4*)(ws + (size_t)i * 16) = ones;
        for (u32 i = gt; i < FLAG_BYTES / 16; i += nt) *(u32x4*)(ws + W_FLAG + (size_t)i * 16) = zero;
        if (gt < 4096) {
            const int i = gt, ii = i & 7, row = (i >> 3) & 15, gA = (i >> 7) & 3, ks = i >> 9, hf = (row >> 1) & 1, m = 2 * (row >> 2) + (row & 1);
            const int d = 128 * (ks >> 1) + 64 * (gA >> 1) + 32 * (gA & 1) + 8 * (ii >> 1) + 4 * (ks & 1) + 2 * hf + (ii & 1);
            const u16 v = p.mem0[m * DM + d];
            ((u16*)(ws + W_MEM))[i] = v; ((u16*)(ws + W_MEM + 8192))[i] = v;
        }
    }
}

__device__ __forceinline__ int preferred_role(u32 xcc, int rank) {
    if (xcc >= 4) {
        const int xl = xcc - 4;
        if (rank < 8) return xl * 8 + rank;
        if (rank < 16) return 32 + xl * 8 + rank - 8;
        return 192 + xl * 16 + rank - 16;
    }
    if (rank < 16) return 64 + (int)xcc * 16 + rank;
    if (rank < 24) return 128 + (int)xcc * 8 + rank - 16;
    return 160 + (int)xcc * 8 + rank - 24;
}

template <int WA, int WD, int WB, int WG, int WV>
__global__ void __launch_bounds__(256, 1) k_main(Params p) {
    extern __shared__ __attribute__((aligned(16))) unsigned char smem[];
    gu32* flags = (gu32*)(p.ws + W_FLAG);
    LAS int* sh = (LAS int*)(smem + L_MISC);
    if (threadIdx.x == 0) {
        int role = preferred_role(xcc_id() & 7u, (int)(blockIdx.x >> 3) & 31);
        const u32 old = __hip_atomic_exchange(flags + F_CLAIM + role, 1u, RLX_AGENT);
        __hip_atomic_fetch_add(flags + F_CTRL + 1, 1u, RLX_AGENT);
        if (old != 0u) {
            role = -1;
            for (u32 spins = 0; __hip_atomic_load(flags + F_CTRL + 1, RLX_AGENT) < gridDim.x && spins < SPIN_CAP; ++spins) __builtin_amdgcn_s_sleep(8);
            for (int r = 0; r < 256; ++r) if (__hip_atomic_exchange(flags + F_CLAIM + r, 1u, RLX_AGENT) == 0u) { role = r; break; }
        }
        *sh = role;
    }
    __syncthreads();
    const int role = __builtin_amdgcn_readfirstlane(*sh);
    __syncthreads();
    if (role < 0 || role >= 192) return;
    if (role < 64) {
        u32 mA = 0xFFFFFFFFu, mB = 0xFFFFFFFFu;
        {
            const int l = threadIdx.x & 31, x = l >> 2, k = l & 3;
            const u32 v = ((const u32*)(p.ws + W_SEL))[l];
            const bool clsA = (((k & 1) ^ (k >> 1)) == 0) == (x >= 4);
            u32 a = clsA ? v : 0xFFFFFFFFu, b = clsA ? 0xFFFFFFFFu : v;
            for (int d = 16; d; d >>= 1) { a = min(a, (u32)__shfl_xor((int)a, d)); b = min(b, (u32)__shfl_xor((int)b, d)); }
            mA = a; mB = b;
        }
        const u32 sel0 = __builtin_amdgcn_readfirstlane(mB < mA ? 1u : 0u);
        if (role < 32) chain_role<true, WA, WD, WB, WG>(p, smem, role, sel0, sel0 ^ 1u);
        else chain_role<false, 0, 0, WV, WG>(p, smem, role - 32, sel0, sel0 ^ 1u);
    } else if (role < 128) {
        const int i = role - 64;
        if (i & 1) proj_role<true>(p, 1, i >> 1); else proj_role<false>(p, 0, i >> 1);
        oproj_role(p, smem, 32 + i);
    } else if (role < 160) {
        proj_role<false>(p, 2, role - 128);
        xattn_role(p, role - 128);
    } else {
        if (role - 160 < 14) { moe_role(p, smem, role - 160); __syncthreads(); }
        oproj_role(p, smem, role - 160);
    }
}

extern "C" void kernel_launch(void* const* d_in, const int* in_sizes, int n_in, void* d_out, int out_size, void* d_ws, size_t ws_size, hipStream_t stream) {
    Params p;
    p.x = (const u16*)d_in[0]; p.mem0 = (const u16*)d_in[1]; p.Wq = (const u16*)d_in[2]; p.Wk = (const u16*)d_in[3]; p.Wv = (const u16*)d_in[4]; p.Wo = (const u16*)d_in[5];
    p.wr = (const u16*)d_in[8]; p.wg = (const u16*)d_in[9]; p.wu = (const u16*)d_in[10]; p.wd = (const u16*)d_in[11];
    p.out = (u16*)d_out;
    p.ws = (unsigned char*)(((uintptr_t)d_ws + (2u << 20) - 1) & ~(uintptr_t)((2u << 20) - 1));
    auto kern = k_main<14, 8, 16, 1, 0>;
    (void)hipFuncSetAttribute((const void*)kern, hipFuncAttributeMaxDynamicSharedMemorySize, (int)LDS_BYTES);
    hipLaunchKernelGGL(k_init, dim3(512), dim3(256), 0, stream, p);
    hipLaunchKernelGGL(kern, dim3(256), dim3(256), LDS_BYTES, stream, p);
}
```
